```python
import torch

def _softmax(x, axis=-1):
    return torch.softmax(x, dim=axis)
def _gelu(x):
    return torch.nn.functional.gelu(x, approximate="tanh")
def _silu(x):
    return torch.nn.functional.silu(x)
def _sigmoid(x):
    return torch.sigmoid(x)
def _layernorm(x, axis=-1, eps=1e-5):
    m = x.mean(dim=axis, keepdim=True)
    v = x.var(dim=axis, keepdim=True, unbiased=False)
    return (x - m) / torch.sqrt(v + eps)
def _scatter_add(tgt, idx, src, axis=0):
    return tgt.clone().index_add_(axis, idx, src)
def _segment_sum(src, seg, n):
    out = torch.zeros((n,) + src.shape[1:], dtype=src.dtype)
    return out.index_add_(0, seg, src)
def _pad(x, pw):
    flat = tuple(v for pair in reversed(pw) for v in pair)
    return torch.nn.functional.pad(x, flat)

kernel_name = 'sequential_preLN__linear_attn_block__dense_activation_squared_relu__prompt'

def setup_inputs():

    _w = (lambda a: (lambda t: t.to(torch.bfloat16) if t.is_floating_point() else t)(torch.as_tensor(a)))
    def _A_setup():
        B, S, D = 1, 2048, 448
        return {
            "x": _w((lambda *s: torch.randn(*s, dtype=torch.bfloat16) * 0.05)(B, S, D)),
            "wqkv": _w((lambda *s: torch.randn(*s, dtype=torch.bfloat16) * 0.1)(D, 3 * D)),
            "wo": _w((lambda *s: torch.randn(*s, dtype=torch.bfloat16) * 0.05)(D, D)),
        }
    _A_in = _A_setup()

    def _B_setup():
        B, S, D, F = 1, 2048, 448, 1792
        return {
            "x": _w((lambda *s: torch.randn(*s, dtype=torch.bfloat16) * 0.05)(B, S, D)),
            "w1": _w((lambda *s: torch.randn(*s, dtype=torch.bfloat16) * 0.05)(D, F)),
            "b1": _w((lambda *s: torch.randn(*s, dtype=torch.bfloat16) * 0.2)(F)),
            "w2": _w((lambda *s: torch.randn(*s, dtype=torch.bfloat16) * 0.05)(F, D)),
            "b2": _w((lambda *s: torch.randn(*s, dtype=torch.bfloat16) * 0.8)(D)),
        }
    _B_in = _B_setup()

    return {
        "A_x": _A_in["x"],
        "A_wqkv": _A_in["wqkv"],
        "A_wo": _A_in["wo"],
        "B_x": _B_in["x"],
        "B_w1": _B_in["w1"],
        "B_b1": _B_in["b1"],
        "B_w2": _B_in["w2"],
        "B_b2": _B_in["b2"],
    }

def reference(A_x, A_wqkv, A_wo, B_x, B_w1, B_b1, B_w2, B_b2):
    def _A_ref(x, wqkv, wo):
        B, S, D = x.shape
        H = 16; Dh = D // H
        qkv = (lambda x, *d: x.permute(*d))((x @ wqkv).reshape(B, S, 3, H, Dh), 2, 0, 3, 1, 4)
        q, k, v = qkv[0], qkv[1], qkv[2]
        phi = lambda t: torch.where(t > 0, t + 1, torch.exp(t)) + 1e-6
        pq, pk = phi(q), phi(k)
        kv = torch.einsum("bhsd,bhse->bhde", pk, v)
        z = torch.einsum("bhsd->bhd", pk)
        num = torch.einsum("bhqd,bhde->bhqe", pq, kv)
        den = torch.einsum("bhqd,bhd->bhq", pq, z)[..., None] + 1e-6
        o = num / den
        return (lambda x, *d: x.permute(*d))(o, 0, 2, 1, 3).reshape(B, S, D) @ wo

    def _B_ref(x, w1, b1, w2, b2):
        h = torch.einsum("bsd,df->bsf", x, w1) + b1
        zero = torch.zeros_like(h)
        r = torch.where(h > zero, h, zero)
        h = r * r
        out = torch.einsum("bsf,fd->bsd", h, w2) + b2
        return out

    x = A_x
    h = x + _A_ref(_layernorm(x, axis=-1), A_wqkv, A_wo)
    h2 = h + _B_ref(_layernorm(h, axis=-1), B_w1, B_b1, B_w2, B_b2)
    return h2
```

```cpp
#include <hip/hip_runtime.h>
#include <hip/hip_bf16.h>
#include <stdint.h>

typedef unsigned short u16;
static constexpr int S=2048, D=448, H=16, DH=28, F=1792;
__device__ __forceinline__ float bf2f(u16 v){ return __uint_as_float(((unsigned)v)<<16); }
__device__ __forceinline__ u16 f2bf(float f){ unsigned u=__float_as_uint(f); u += 0x7fffu + ((u>>16)&1u); return (u16)(u>>16); }

template<typename T> __device__ __forceinline__ float ld(const T* p, int i);
template<> __device__ __forceinline__ float ld<u16>(const u16* p,int i){return bf2f(p[i]);}
template<> __device__ __forceinline__ float ld<float>(const float* p,int i){return p[i];}

template<typename T>
__global__ void ln_k(const T* x, float* y){
  int r=blockIdx.x; int t=threadIdx.x;
  float v[7]; float s=0;
  for(int i=0;i<7;i++){ v[i]=ld<T>(x, r*D+t+64*i); s+=v[i]; }
  for(int m=32;m>=1;m>>=1) s+=__shfl_xor(s,m);
  float mean=s/D; float q=0;
  for(int i=0;i<7;i++){ float d=v[i]-mean; q+=d*d; }
  for(int m=32;m>=1;m>>=1) q+=__shfl_xor(q,m);
  float rs=1.0f/sqrtf(q/D+1e-5f);
  for(int i=0;i<7;i++) y[r*D+t+64*i]=(v[i]-mean)*rs;
}
__global__ void gemm_k(const float* A, const u16* W, float* C, int N, int K){
  __shared__ float As[16][65];
  __shared__ float Ws[64][17];
  int tx=threadIdx.x&15, ty=threadIdx.x>>4;
  int r0=blockIdx.y*16, c0=blockIdx.x*16;
  float acc=0;
  for(int k0=0;k0<K;k0+=64){
    for(int i=0;i<4;i++){ int idx=threadIdx.x+256*i; int rr=idx>>6, kk=idx&63; As[rr][kk]=A[(r0+rr)*K+k0+kk]; }
    for(int i=0;i<4;i++){ int idx=threadIdx.x+256*i; int kk=idx>>4, cc=idx&15; Ws[kk][cc]=bf2f(W[(k0+kk)*N+c0+cc]); }
    __syncthreads();
    for(int k=0;k<64;k++) acc+=As[ty][k]*Ws[k][tx];
    __syncthreads();
  }
  C[(r0+ty)*N+c0+tx]=acc;
}
__device__ __forceinline__ float phi(float t){ return (t>0? t+1.f: expf(t))+1e-6f; }
__global__ void kv_k(const float* qkv, float* kv, float* z){
  int h=blockIdx.x/DH, d=blockIdx.x%DH;
  __shared__ float red[256][29];
  float acc[29];
  for(int e=0;e<29;e++) acc[e]=0;
  for(int s=threadIdx.x;s<S;s+=256){
    float pk=phi(qkv[s*3*D + D + h*DH+d]);
    #pragma unroll
    for(int e=0;e<DH;e++) acc[e]+=pk*qkv[s*3*D+2*D+h*DH+e];
    acc[28]+=pk;
  }
  #pragma unroll
  for(int e=0;e<29;e++) red[threadIdx.x][e]=acc[e];
  __syncthreads();
  if(threadIdx.x<29){ float s=0; for(int i=0;i<256;i++) s+=red[i][threadIdx.x];
    if(threadIdx.x<28) kv[(h*DH+d)*DH+threadIdx.x]=s; else z[h*DH+d]=s; }
}
__global__ void o_k(const float* qkv, const float* kv, const float* z, float* o){
  int s=blockIdx.x; int c=threadIdx.x;
  int h=c/DH, e=c%DH;
  float num=0, den=0;
  for(int d=0;d<DH;d++){ float pq=phi(qkv[s*3*D+h*DH+d]); num+=pq*kv[(h*DH+d)*DH+e]; den+=pq*z[h*DH+d]; }
  o[s*D+c]=num/(den+1e-6f);
}
__global__ void res1_k(const u16* x, const float* a, float* h){ int i=blockIdx.x*256+threadIdx.x; h[i]=bf2f(x[i])+a[i]; }
__global__ void act_k(float* h1, const u16* b1){ int i=blockIdx.x*256+threadIdx.x; float v=h1[i]+bf2f(b1[i%F]); v=v>0?v:0; h1[i]=v*v; }
__global__ void fin_k(const float* h, const float* f, const u16* b2, u16* out){ int i=blockIdx.x*256+threadIdx.x; out[i]=f2bf(h[i]+f[i]+bf2f(b2[i%D])); }

extern "C" void kernel_launch(void* const* d_in, const int* in_sizes, int n_in,
                              void* d_out, int out_size, void* d_ws, size_t ws_size,
                              hipStream_t stream) {
  const u16* x=(const u16*)d_in[0]; const u16* wqkv=(const u16*)d_in[1]; const u16* wo=(const u16*)d_in[2];
  const u16* w1=(const u16*)d_in[4]; const u16* b1=(const u16*)d_in[5]; const u16* w2=(const u16*)d_in[6]; const u16* b2=(const u16*)d_in[7];
  float* ws=(float*)d_ws;
  float* xn=ws; ws+=S*D; float* qkv=ws; ws+=S*3*D; float* kv=ws; ws+=H*DH*DH; float* z=ws; ws+=H*DH; float* o=ws; ws+=S*D;
  float* a=ws; ws+=S*D; float* h=ws; ws+=S*D; float* hn=ws; ws+=S*D; float* h1=ws; ws+=S*F; float* f=ws; ws+=S*D;
  ln_k<u16><<<S,64,0,stream>>>(x,xn);
  gemm_k<<<dim3(3*D/16,S/16),256,0,stream>>>(xn,wqkv,qkv,3*D,D);
  kv_k<<<H*DH,256,0,stream>>>(qkv,kv,z);
  o_k<<<S,D,0,stream>>>(qkv,kv,z,o);
  gemm_k<<<dim3(D/16,S/16),256,0,stream>>>(o,wo,a,D,D);
  res1_k<<<S*D/256,256,0,stream>>>(x,a,h);
  ln_k<float><<<S,64,0,stream>>>(h,hn);
  gemm_k<<<dim3(F/16,S/16),256,0,stream>>>(hn,w1,h1,F,D);
  act_k<<<S*F/256,256,0,stream>>>(h1,b1);
  gemm_k<<<dim3(D/16,S/16),256,0,stream>>>(h1,w2,f,D,F);
  fin_k<<<S*D/256,256,0,stream>>>(h,f,b2,(u16*)d_out);
}
```

```cpp
#include <hip/hip_runtime.h>
#include <hip/hip_bf16.h>

__global__ void __launch_bounds__(256, 1)
fused_block_kernel(const void* x, const void* wqkv, const void* wo, const void* w1, const void* b1,
                   const void* w2, const void* b2, void* out, void* ws) {
  asm volatile(
      "s_load_dwordx2 s[12:13], %0, 0x8\n"
      "s_load_dwordx2 s[8:9], %0, 0x0\n"
      "v_lshrrev_b32 v2, 6, %2\n"
      "v_and_b32 v1, 63, %2\n"
      "v_readfirstlane_b32 s33, v2\n"
      "s_and_b32 s42, %1, 7\n"
      "s_lshr_b32 s43, %1, 3\n"
      "s_and_b32 s45, s43, 3\n"
      "s_lshr_b32 s44, s43, 2\n"
      "s_and_b32 s46, s42, 3\n"
      "s_lshl2_add_u32 s45, s46, s45\n"
      "s_lshr_b32 s46, s42, 2\n"
      "s_lshl3_add_u32 s44, s46, s44\n"
      "s_add_u32 s47, s44, s45\n"
      "s_mul_i32 s46, s47, 37\n"
      "s_lshr_b32 s46, s46, 8\n"
      "s_mul_i32 s46, s46, 7\n"
      "s_sub_u32 s47, s47, s46\n"
      "s_lshl_b32 s49, s47, 7\n"
      "s_mul_i32 s56, s49, 1344\n"
      "s_mov_b32 s14, 0x126000\n"
      "s_mov_b32 s15, 0x20000\n"
      "s_mov_b32 s10, 0x1c0000\n"
      "s_mov_b32 s11, 0x20000\n"
      "v_lshrrev_b32 v11, 2, v1\n"
      "v_and_b32 v12, 3, v1\n"
      "v_lshlrev_b32 v2, 4, v12\n"
      "v_min_u32 v2, 40, v2\n"
      "v_mul_u32_u24 v7, 2688, v11\n"
      "s_mul_i32 s46, s33, 43008\n"
      "s_mul_i32 s47, s45, 56\n"
      "s_add_u32 s46, s46, s47\n"
      "s_add_u32 s46, s46, 896\n"
      "v_add3_u32 v7, v7, v2, s46\n"
      "v_add_u32 v8, 896, v7\n"
      "s_lshl_b32 s64, s33, 10\n"
      "v_lshrrev_b32 v9, 3, v1\n"
      "v_lshrrev_b32 v10, 4, v1\n"
      "v_and_b32 v2, 7, v1\n"
      "v_xor_b32 v2, v2, v10\n"
      "v_mul_u32_u24 v13, 896, v9\n"
      "v_lshl_add_u32 v13, v2, 4, v13\n"
      "s_lshl2_add_u32 s65, s44, s33\n"
      "s_mul_i32 s46, s65, 28672\n"
      "v_add_u32 v3, s46, v13\n"
      "s_mul_i32 s63, s33, 12288\n"
      "s_add_u32 s63, s63, 57344\n"
      "s_waitcnt lgkmcnt(0)\n"
      "s_mov_b32 m0, s64\n"
      "v_xor_b32 v4, 64, v3\n"
      "buffer_load_dwordx4 v7, s[12:15], s56 offen lds\n"
      "s_add_u32 m0, s64, 28672\n"
      "v_add_u32 v4, 7168, v4\n"
      "buffer_load_dwordx4 v8, s[12:15], s56 offen lds\n"
      "s_mov_b32 m0, s63\n"
      "v_add_u32 v5, 14336, v3\n"
      "buffer_load_dwordx4 v3, s[8:11], s49 offen lds\n"
      "s_add_u32 m0, s63, 1024\n"
      "v_add_u32 v6, 14336, v4\n"
      "buffer_load_dwordx4 v4, s[8:11], s49 offen lds\n"
      "s_add_u32 m0, s63, 2048\n"
      "s_nop 0\n"
      "buffer_load_dwordx4 v5, s[8:11], s49 offen lds\n"
      "s_add_u32 m0, s63, 3072\n"
      "s_nop 0\n"
      "buffer_load_dwordx4 v6, s[8:11], s49 offen lds\n"
      "s_add_u32 s50, s49, 128\n"
      "s_cmp_eq_u32 s50, 896\n"
      "s_cselect_b32 s50, 0, s50\n"
      "s_mul_i32 s57, s50, 1344\n"
      "s_add_u32 m0, s64, 4096\n"
      "s_load_dwordx2 s[36:37], %0, 0x40\n"
      "buffer_load_dwordx4 v7, s[12:15], s57 offen lds\n"
      "s_add_u32 m0, s64, 32768\n"
      "s_load_dwordx2 s[16:17], %0, 0x10\n"
      "buffer_load_dwordx4 v8, s[12:15], s57 offen lds\n"
      "s_add_u32 m0, s63, 4096\n"
      "s_load_dwordx2 s[20:21], %0, 0x18\n"
      "buffer_load_dwordx4 v3, s[8:11], s50 offen lds\n"
      "s_add_u32 m0, s63, 5120\n"
      "s_load_dwordx2 s[34:35], %0, 0x20\n"
      "buffer_load_dwordx4 v4, s[8:11], s50 offen lds\n"
      "s_add_u32 m0, s63, 6144\n"
      "s_load_dwordx2 s[24:25], %0, 0x28\n"
      "buffer_load_dwordx4 v5, s[8:11], s50 offen lds\n"
      "s_add_u32 m0, s63, 7168\n"
      "s_load_dwordx2 s[40:41], %0, 0x30\n"
      "buffer_load_dwordx4 v6, s[8:11], s50 offen lds\n"
      "s_load_dwordx2 s[28:29], %0, 0x38\n"
      "s_getpc_b64 s[68:69]\n"
      ".Ltouch_%=:\n"
      "s_and_b32 s68, s68, 0xffffff80\n"
      "s_lshl2_add_u32 s46, s43, s33\n"
      "s_lshl_b32 s46, s46, 1\n"
      "v_and_b32 v2, 1, v1\n"
      "v_add_lshl_u32 v2, s46, v2, 7\n"
      "s_mov_b32 s46, .Lcodeend_%=-.Ltouch_%=\n"
      "v_min_u32 v2, s46, v2\n"
      "global_load_dword v14, v2, s[68:69]\n"
      "s_mov_b32 s18, 0x62000\n"
      "s_mov_b32 s19, 0x20000\n"
      "s_mov_b32 s22, 0x188000\n"
      "s_mov_b32 s23, 0x20000\n"
      "s_mov_b32 s26, 0x188000\n"
      "s_mov_b32 s27, 0x20000\n"
      "s_mov_b32 s30, 0x1c0000\n"
      "s_mov_b32 s31, 0x20000\n"
      "s_mov_b32 s38, 0xd80000\n"
      "s_mov_b32 s39, 0x20000\n"
      "v_mov_b32 v2, 0\n"
      "s_waitcnt lgkmcnt(0)\n"
      "buffer_load_dword v15, v2, s[36:39], 0 offen sc1\n"
      "s_add_u32 s51, s50, 128\n"
      "s_cmp_eq_u32 s51, 896\n"
      "s_cselect_b32 s51, 0, s51\n"
      "s_mul_i32 s58, s51, 1344\n"
      "s_add_u32 s52, s51, 128\n"
      "s_cmp_eq_u32 s52, 896\n"
      "s_cselect_b32 s52, 0, s52\n"
      "s_mul_i32 s59, s52, 1344\n"
      "s_add_u32 s53, s52, 128\n"
      "s_cmp_eq_u32 s53, 896\n"
      "s_cselect_b32 s53, 0, s53\n"
      "s_mul_i32 s60, s53, 1344\n"
      "s_add_u32 s54, s53, 128\n"
      "s_cmp_eq_u32 s54, 896\n"
      "s_cselect_b32 s54, 0, s54\n"
      "s_mul_i32 s61, s54, 1344\n"
      "s_add_u32 s55, s54, 128\n"
      "s_cmp_eq_u32 s55, 896\n"
      "s_cselect_b32 s55, 0, s55\n"
      "s_mul_i32 s62, s55, 1344\n"
      "v_and_b32 v16, 31, v1\n"
      "v_lshrrev_b32 v17, 5, v1\n"
      "v_bfe_u32 v2, v1, 1, 3\n"
      "v_xor_b32 v2, v2, v17\n"
      "s_lshl_b32 s46, s33, 1\n"
      "v_xor_b32 v2, s46, v2\n"
      "v_lshlrev_b32 v2, 4, v2\n"
      "v_lshl_add_u32 v2, v16, 7, v2\n"
      "v_add_u32 v18, s63, v2\n"
      "v_xor_b32 v19, 32, v18\n"
      "v_xor_b32 v20, 64, v18\n"
      "v_xor_b32 v21, 96, v18\n"
      "v_and_b32 v22, 3, v11\n"
      "v_and_b32 v2, 1, v10\n"
      "v_lshl_add_u32 v23, v2, 2, v12\n"
      "v_cmp_le_u32 vcc, 6, v23\n"
      "v_xor_b32 v2, 1, v23\n"
      "v_lshl_add_u32 v24, v17, 3, v22\n"
      "v_cndmask_b32 v2, v23, v2, vcc\n"
      "v_lshl_add_u32 v2, v2, 3, s64\n"
      "v_lshl_add_u32 v24, v24, 6, v2\n"
      "v_xor_b32 v25, 1024, v24\n"
      "v_xor_b32 v26, 2048, v24\n"
      "v_xor_b32 v27, 3072, v24\n"
      "v_lshlrev_b32 v28, 4, v1\n"
      "s_mov_b32 s67, 0x3f803f80\n"
      "v_xor_b32 v2, v11, v10\n"
      "v_and_b32 v2, 1, v2\n"
      "v_add_u32 v2, -1, v2\n"
      "v_and_b32 v30, s67, v2\n"
      "v_and_b32 v31, s67, v2\n"
      "v_and_b32 v32, s67, v2\n"
      "v_and_b32 v33, s67, v2\n"
      "s_min_u32 s46, s43, 27\n"
      "s_mul_i32 s72, s46, 37\n"
      "s_lshr_b32 s72, s72, 8\n"
      "s_mul_i32 s47, s72, 7\n"
      "s_sub_u32 s71, s46, s47\n"
      "s_lshr_b32 s46, s42, 1\n"
      "s_lshl2_add_u32 s68, s46, s72\n"
      "s_and_b32 s46, s42, 1\n"
      "s_lshl1_add_u32 s70, s68, s46\n"
      "s_mul_i32 s46, s46, 7\n"
      "s_add_u32 s69, s46, s71\n"
      "v_mov_b32 v11, 0\n"
      "v_mov_b32 v29, 0\n"
      "v_mov_b32 v104, s67\n"
      "v_mov_b32 v105, s67\n"
      "v_mov_b32 v106, s67\n"
      "v_mov_b32 v107, s67\n"
      "v_mov_b32 v108, s67\n"
      "v_mov_b32 v109, s67\n"
      "v_mov_b32 v110, s67\n"
      "v_mov_b32 v111, s67\n"
      "v_mov_b32 v42, 0x35c1a2a0\n"
      "v_mov_b32 v43, 0x3f317218\n"
      "s_mov_b32 s74, 0\n"
      "s_mov_b32 s75, -1\n"
      "s_lshl_b32 s46, s33, 8\n"
      "s_add_u32 s46, s46, 159744\n"
      "v_lshl_add_u32 v44, v1, 2, s46\n"
      "v_lshl_add_u32 v45, v17, 4, s46\n"
      "s_lshl2_add_u32 s46, s33, 160768\n"
      "v_add_u32 v46, s46, v28\n"
      "v_lshlrev_b32 v47, 4, v16\n"
      "v_add_u32 v47, 160768, v47\n"
      "s_waitcnt vmcnt(8)\n"
      "s_barrier\n"
      "ds_read_b128 v[144:147], v18 offset:0\n"
      "ds_read_b64_tr_b16 a[0:1], v24 offset:0\n"
      "ds_read_b64_tr_b16 a[2:3], v24 offset:256\n"
      "ds_read_b64_tr_b16 a[4:5], v24 offset:28672\n"
      "ds_read_b64_tr_b16 a[6:7], v24 offset:28928\n"
      "s_add_u32 m0, s64, 8192\n"
      "ds_read_b128 v[148:151], v19 offset:0\n"
      "buffer_load_dwordx4 v7, s[12:15], s58 offen lds\n"
      "ds_read_b64_tr_b16 a[8:9], v25 offset:0\n"
      "ds_read_b64_tr_b16 a[10:11], v25 offset:256\n"
      "ds_read_b64_tr_b16 a[12:13], v25 offset:28672\n"
      "ds_read_b64_tr_b16 a[14:15], v25 offset:28928\n"
      "s_add_u32 m0, s64, 36864\n"
      "ds_read_b128 v[152:155], v20 offset:0\n"
      "buffer_load_dwordx4 v8, s[12:15], s58 offen lds\n"
      "s_waitcnt lgkmcnt(8)\n"
      "v_mfma_f32_32x32x16_bf16 v[128:143], v[144:147], a[0:3], 0\n"
      "ds_read_b64_tr_b16 a[16:17], v26 offset:0\n"
      "ds_read_b64_tr_b16 a[18:19], v26 offset:256\n"
      "ds_read_b64_tr_b16 a[20:21], v26 offset:28672\n"
      "ds_read_b64_tr_b16 a[22:23], v26 offset:28928\n"
      "v_mfma_f32_16x16x32_bf16 v[34:37], v[30:33], a[0:3], 0\n"
      "s_waitcnt lgkmcnt(10)\n"
      "v_mfma_f32_32x32x16_bf16 v[112:127], v[144:147], a[4:7], 0\n"
      "s_add_u32 m0, s63, 8192\n"
      "ds_read_b128 v[156:159], v21 offset:0\n"
      "buffer_load_dwordx4 v3, s[8:11], s51 offen lds\n"
      "ds_read_b64_tr_b16 a[24:25], v27 offset:0\n"
      "ds_read_b64_tr_b16 a[26:27], v27 offset:256\n"
      "v_mfma_f32_16x16x32_bf16 v[38:41], v[30:33], a[4:7], 0\n"
      "s_waitcnt lgkmcnt(10)\n"
      "v_mfma_f32_32x32x16_bf16 v[128:143], v[148:151], a[8:11], v[128:143]\n"
      "ds_read_b64_tr_b16 a[28:29], v27 offset:28672\n"
      "ds_read_b64_tr_b16 a[30:31], v27 offset:28928\n"
      "s_waitcnt lgkmcnt(10)\n"
      "v_mfma_f32_32x32x16_bf16 v[112:127], v[148:151], a[12:15], v[112:127]\n"
      "s_add_u32 m0, s63, 9216\n"
      "s_nop 0\n"
      "buffer_load_dwordx4 v4, s[8:11], s51 offen lds\n"
      "s_waitcnt lgkmcnt(7)\n"
      "v_mfma_f32_32x32x16_bf16 v[128:143], v[152:155], a[16:19], v[128:143]\n"
      "s_waitcnt lgkmcnt(5)\n"
      "v_mfma_f32_32x32x16_bf16 v[112:127], v[152:155], a[20:23], v[112:127]\n"
      "s_add_u32 m0, s63, 10240\n"
      "s_nop 0\n"
      "buffer_load_dwordx4 v5, s[8:11], s51 offen lds\n"
      "s_waitcnt lgkmcnt(2)\n"
      "v_mfma_f32_32x32x16_bf16 v[128:143], v[156:159], a[24:27], v[128:143]\n"
      "s_waitcnt lgkmcnt(0)\n"
      "v_mfma_f32_32x32x16_bf16 v[112:127], v[156:159], a[28:31], v[112:127]\n"
      "s_add_u32 m0, s63, 11264\n"
      "s_nop 0\n"
      "buffer_load_dwordx4 v6, s[8:11], s51 offen lds\n"
      "s_waitcnt vmcnt(8)\n"
      "s_barrier\n"
      "ds_read_b128 v[160:163], v18 offset:4096\n"
      "ds_read_b64_tr_b16 a[0:1], v24 offset:4096\n"
      "ds_read_b64_tr_b16 a[2:3], v24 offset:4352\n"
      "ds_read_b64_tr_b16 a[4:5], v24 offset:32768\n"
      "ds_read_b64_tr_b16 a[6:7], v24 offset:33024\n"
      "s_add_u32 m0, s64, 12288\n"
      "ds_read_b128 v[164:167], v19 offset:4096\n"
      "buffer_load_dwordx4 v7, s[12:15], s59 offen lds\n"
      "ds_read_b64_tr_b16 a[8:9], v25 offset:4096\n"
      "ds_read_b64_tr_b16 a[10:11], v25 offset:4352\n"
      "ds_read_b64_tr_b16 a[12:13], v25 offset:32768\n"
      "ds_read_b64_tr_b16 a[14:15], v25 offset:33024\n"
      "v_dot2c_f32_bf16 v11, s67, v144\n"
      "v_dot2c_f32_bf16 v29, v144, v144\n"
      "v_dot2c_f32_bf16 v11, s67, v145\n"
      "v_dot2c_f32_bf16 v29, v145, v145\n"
      "v_dot2c_f32_bf16 v11, s67, v146\n"
      "v_dot2c_f32_bf16 v29, v146, v146\n"
      "v_dot2c_f32_bf16 v11, s67, v147\n"
      "v_dot2c_f32_bf16 v29, v147, v147\n"
      "v_dot2c_f32_bf16 v11, s67, v148\n"
      "v_dot2c_f32_bf16 v29, v148, v148\n"
      "v_dot2c_f32_bf16 v11, s67, v149\n"
      "v_dot2c_f32_bf16 v29, v149, v149\n"
      "v_dot2c_f32_bf16 v11, s67, v150\n"
      "v_dot2c_f32_bf16 v29, v150, v150\n"
      "v_dot2c_f32_bf16 v11, s67, v151\n"
      "v_dot2c_f32_bf16 v29, v151, v151\n"
      "s_add_u32 m0, s64, 40960\n"
      "ds_read_b128 v[168:171], v20 offset:4096\n"
      "buffer_load_dwordx4 v8, s[12:15], s59 offen lds\n"
      "v_dot2c_f32_bf16 v11, s67, v152\n"
      "v_dot2c_f32_bf16 v29, v152, v152\n"
      "v_dot2c_f32_bf16 v11, s67, v153\n"
      "v_dot2c_f32_bf16 v29, v153, v153\n"
      "v_dot2c_f32_bf16 v11, s67, v154\n"
      "v_dot2c_f32_bf16 v29, v154, v154\n"
      "v_dot2c_f32_bf16 v11, s67, v155\n"
      "v_dot2c_f32_bf16 v29, v155, v155\n"
      "v_dot2c_f32_bf16 v11, s67, v156\n"
      "v_dot2c_f32_bf16 v29, v156, v156\n"
      "v_dot2c_f32_bf16 v11, s67, v157\n"
      "v_dot2c_f32_bf16 v29, v157, v157\n"
      "v_dot2c_f32_bf16 v11, s67, v158\n"
      "v_dot2c_f32_bf16 v29, v158, v158\n"
      "v_dot2c_f32_bf16 v11, s67, v159\n"
      "v_dot2c_f32_bf16 v29, v159, v159\n"
      "s_waitcnt lgkmcnt(8)\n"
      "v_mfma_f32_32x32x16_bf16 v[128:143], v[160:163], a[0:3], v[128:143]\n"
      "ds_read_b64_tr_b16 a[16:17], v26 offset:4096\n"
      "ds_read_b64_tr_b16 a[18:19], v26 offset:4352\n"
      "ds_read_b64_tr_b16 a[20:21], v26 offset:32768\n"
      "ds_read_b64_tr_b16 a[22:23], v26 offset:33024\n"
      "v_mfma_f32_16x16x32_bf16 v[34:37], v[30:33], a[0:3], v[34:37]\n"
      "s_waitcnt lgkmcnt(10)\n"
      "v_mfma_f32_32x32x16_bf16 v[112:127], v[160:163], a[4:7], v[112:127]\n"
      "s_mov_b32 m0, s63\n"
      "ds_read_b128 v[172:175], v21 offset:4096\n"
      "buffer_load_dwordx4 v3, s[8:11], s52 offen lds\n"
      "ds_read_b64_tr_b16 a[24:25], v27 offset:4096\n"
      "ds_read_b64_tr_b16 a[26:27], v27 offset:4352\n"
      "v_mfma_f32_16x16x32_bf16 v[38:41], v[30:33], a[4:7], v[38:41]\n"
      "s_waitcnt lgkmcnt(10)\n"
      "v_mfma_f32_32x32x16_bf16 v[128:143], v[164:167], a[8:11], v[128:143]\n"
      "ds_read_b64_tr_b16 a[28:29], v27 offset:32768\n"
      "ds_read_b64_tr_b16 a[30:31], v27 offset:33024\n"
      "s_waitcnt lgkmcnt(10)\n"
      "v_mfma_f32_32x32x16_bf16 v[112:127], v[164:167], a[12:15], v[112:127]\n"
      "s_add_u32 m0, s63, 1024\n"
      "s_nop 0\n"
      "buffer_load_dwordx4 v4, s[8:11], s52 offen lds\n"
      "s_waitcnt lgkmcnt(7)\n"
      "v_mfma_f32_32x32x16_bf16 v[128:143], v[168:171], a[16:19], v[128:143]\n"
      "s_waitcnt lgkmcnt(5)\n"
      "v_mfma_f32_32x32x16_bf16 v[112:127], v[168:171], a[20:23], v[112:127]\n"
      "s_add_u32 m0, s63, 2048\n"
      "s_nop 0\n"
      "buffer_load_dwordx4 v5, s[8:11], s52 offen lds\n"
      "s_waitcnt lgkmcnt(2)\n"
      "v_mfma_f32_32x32x16_bf16 v[128:143], v[172:175], a[24:27], v[128:143]\n"
      "s_waitcnt lgkmcnt(0)\n"
      "v_mfma_f32_32x32x16_bf16 v[112:127], v[172:175], a[28:31], v[112:127]\n"
      "s_add_u32 m0, s63, 3072\n"
      "s_nop 0\n"
      "buffer_load_dwordx4 v6, s[8:11], s52 offen lds\n"
      "s_waitcnt vmcnt(6)\n"
      "s_barrier\n"
      "ds_read_b128 v[176:179], v18 offset:8192\n"
      "ds_read_b64_tr_b16 a[0:1], v24 offset:8192\n"
      "ds_read_b64_tr_b16 a[2:3], v24 offset:8448\n"
      "ds_read_b64_tr_b16 a[4:5], v24 offset:36864\n"
      "ds_read_b64_tr_b16 a[6:7], v24 offset:37120\n"
      "s_add_u32 m0, s64, 16384\n"
      "ds_read_b128 v[180:183], v19 offset:8192\n"
      "buffer_load_dwordx4 v7, s[12:15], s60 offen lds\n"
      "ds_read_b64_tr_b16 a[8:9], v25 offset:8192\n"
      "ds_read_b64_tr_b16 a[10:11], v25 offset:8448\n"
      "ds_read_b64_tr_b16 a[12:13], v25 offset:36864\n"
      "ds_read_b64_tr_b16 a[14:15], v25 offset:37120\n"
      "v_dot2c_f32_bf16 v11, s67, v160\n"
      "v_dot2c_f32_bf16 v29, v160, v160\n"
      "v_dot2c_f32_bf16 v11, s67, v161\n"
      "v_dot2c_f32_bf16 v29, v161, v161\n"
      "v_dot2c_f32_bf16 v11, s67, v162\n"
      "v_dot2c_f32_bf16 v29, v162, v162\n"
      "v_dot2c_f32_bf16 v11, s67, v163\n"
      "v_dot2c_f32_bf16 v29, v163, v163\n"
      "v_dot2c_f32_bf16 v11, s67, v164\n"
      "v_dot2c_f32_bf16 v29, v164, v164\n"
      "v_dot2c_f32_bf16 v11, s67, v165\n"
      "v_dot2c_f32_bf16 v29, v165, v165\n"
      "v_dot2c_f32_bf16 v11, s67, v166\n"
      "v_dot2c_f32_bf16 v29, v166, v166\n"
      "v_dot2c_f32_bf16 v11, s67, v167\n"
      "v_dot2c_f32_bf16 v29, v167, v167\n"
      "s_add_u32 m0, s64, 45056\n"
      "ds_read_b128 v[184:187], v20 offset:8192\n"
      "buffer_load_dwordx4 v8, s[12:15], s60 offen lds\n"
      "v_dot2c_f32_bf16 v11, s67, v168\n"
      "v_dot2c_f32_bf16 v29, v168, v168\n"
      "v_dot2c_f32_bf16 v11, s67, v169\n"
      "v_dot2c_f32_bf16 v29, v169, v169\n"
      "v_dot2c_f32_bf16 v11, s67, v170\n"
      "v_dot2c_f32_bf16 v29, v170, v170\n"
      "v_dot2c_f32_bf16 v11, s67, v171\n"
      "v_dot2c_f32_bf16 v29, v171, v171\n"
      "v_dot2c_f32_bf16 v11, s67, v172\n"
      "v_dot2c_f32_bf16 v29, v172, v172\n"
      "v_dot2c_f32_bf16 v11, s67, v173\n"
      "v_dot2c_f32_bf16 v29, v173, v173\n"
      "v_dot2c_f32_bf16 v11, s67, v174\n"
      "v_dot2c_f32_bf16 v29, v174, v174\n"
      "v_dot2c_f32_bf16 v11, s67, v175\n"
      "v_dot2c_f32_bf16 v29, v175, v175\n"
      "s_waitcnt lgkmcnt(8)\n"
      "v_mfma_f32_32x32x16_bf16 v[128:143], v[176:179], a[0:3], v[128:143]\n"
      "ds_read_b64_tr_b16 a[16:17], v26 offset:8192\n"
      "ds_read_b64_tr_b16 a[18:19], v26 offset:8448\n"
      "ds_read_b64_tr_b16 a[20:21], v26 offset:36864\n"
      "ds_read_b64_tr_b16 a[22:23], v26 offset:37120\n"
      "v_mfma_f32_16x16x32_bf16 v[34:37], v[30:33], a[0:3], v[34:37]\n"
      "s_waitcnt lgkmcnt(10)\n"
      "v_mfma_f32_32x32x16_bf16 v[112:127], v[176:179], a[4:7], v[112:127]\n"
      "s_add_u32 m0, s63, 4096\n"
      "ds_read_b128 v[188:191], v21 offset:8192\n"
      "buffer_load_dwordx4 v3, s[8:11], s53 offen lds\n"
      "ds_read_b64_tr_b16 a[24:25], v27 offset:8192\n"
      "ds_read_b64_tr_b16 a[26:27], v27 offset:8448\n"
      "v_mfma_f32_16x16x32_bf16 v[38:41], v[30:33], a[4:7], v[38:41]\n"
      "s_waitcnt lgkmcnt(10)\n"
      "v_mfma_f32_32x32x16_bf16 v[128:143], v[180:183], a[8:11], v[128:143]\n"
      "ds_read_b64_tr_b16 a[28:29], v27 offset:36864\n"
      "ds_read_b64_tr_b16 a[30:31], v27 offset:37120\n"
      "s_waitcnt lgkmcnt(10)\n"
      "v_mfma_f32_32x32x16_bf16 v[112:127], v[180:183], a[12:15], v[112:127]\n"
      "s_add_u32 m0, s63, 5120\n"
      "s_nop 0\n"
      "buffer_load_dwordx4 v4, s[8:11], s53 offen lds\n"
      "s_waitcnt lgkmcnt(7)\n"
      "v_mfma_f32_32x32x16_bf16 v[128:143], v[184:187], a[16:19], v[128:143]\n"
      "s_waitcnt lgkmcnt(5)\n"
      "v_mfma_f32_32x32x16_bf16 v[112:127], v[184:187], a[20:23], v[112:127]\n"
      "s_add_u32 m0, s63, 6144\n"
      "s_nop 0\n"
      "buffer_load_dwordx4 v5, s[8:11], s53 offen lds\n"
      "s_waitcnt lgkmcnt(2)\n"
      "v_mfma_f32_32x32x16_bf16 v[128:143], v[188:191], a[24:27], v[128:143]\n"
      "s_waitcnt lgkmcnt(0)\n"
      "v_mfma_f32_32x32x16_bf16 v[112:127], v[188:191], a[28:31], v[112:127]\n"
      "s_add_u32 m0, s63, 7168\n"
      "s_nop 0\n"
      "buffer_load_dwordx4 v6, s[8:11], s53 offen lds\n"
      "s_waitcnt vmcnt(6)\n"
      "s_barrier\n"
      "ds_read_b128 v[192:195], v18 offset:0\n"
      "ds_read_b64_tr_b16 a[0:1], v24 offset:12288\n"
      "ds_read_b64_tr_b16 a[2:3], v24 offset:12544\n"
      "ds_read_b64_tr_b16 a[4:5], v24 offset:40960\n"
      "ds_read_b64_tr_b16 a[6:7], v24 offset:41216\n"
      "s_add_u32 m0, s64, 20480\n"
      "ds_read_b128 v[196:199], v19 offset:0\n"
      "buffer_load_dwordx4 v7, s[12:15], s61 offen lds\n"
      "ds_read_b64_tr_b16 a[8:9], v25 offset:12288\n"
      "ds_read_b64_tr_b16 a[10:11], v25 offset:12544\n"
      "ds_read_b64_tr_b16 a[12:13], v25 offset:40960\n"
      "ds_read_b64_tr_b16 a[14:15], v25 offset:41216\n"
      "v_dot2c_f32_bf16 v11, s67, v176\n"
      "v_dot2c_f32_bf16 v29, v176, v176\n"
      "v_dot2c_f32_bf16 v11, s67, v177\n"
      "v_dot2c_f32_bf16 v29, v177, v177\n"
      "v_dot2c_f32_bf16 v11, s67, v178\n"
      "v_dot2c_f32_bf16 v29, v178, v178\n"
      "v_dot2c_f32_bf16 v11, s67, v179\n"
      "v_dot2c_f32_bf16 v29, v179, v179\n"
      "v_dot2c_f32_bf16 v11, s67, v180\n"
      "v_dot2c_f32_bf16 v29, v180, v180\n"
      "v_dot2c_f32_bf16 v11, s67, v181\n"
      "v_dot2c_f32_bf16 v29, v181, v181\n"
      "v_dot2c_f32_bf16 v11, s67, v182\n"
      "v_dot2c_f32_bf16 v29, v182, v182\n"
      "v_dot2c_f32_bf16 v11, s67, v183\n"
      "v_dot2c_f32_bf16 v29, v183, v183\n"
      "s_add_u32 m0, s64, 49152\n"
      "ds_read_b128 v[200:203], v20 offset:0\n"
      "buffer_load_dwordx4 v8, s[12:15], s61 offen lds\n"
      "v_dot2c_f32_bf16 v11, s67, v184\n"
      "v_dot2c_f32_bf16 v29, v184, v184\n"
      "v_dot2c_f32_bf16 v11, s67, v185\n"
      "v_dot2c_f32_bf16 v29, v185, v185\n"
      "v_dot2c_f32_bf16 v11, s67, v186\n"
      "v_dot2c_f32_bf16 v29, v186, v186\n"
      "v_dot2c_f32_bf16 v11, s67, v187\n"
      "v_dot2c_f32_bf16 v29, v187, v187\n"
      "v_dot2c_f32_bf16 v11, s67, v188\n"
      "v_dot2c_f32_bf16 v29, v188, v188\n"
      "v_dot2c_f32_bf16 v11, s67, v189\n"
      "v_dot2c_f32_bf16 v29, v189, v189\n"
      "v_dot2c_f32_bf16 v11, s67, v190\n"
      "v_dot2c_f32_bf16 v29, v190, v190\n"
      "v_dot2c_f32_bf16 v11, s67, v191\n"
      "v_dot2c_f32_bf16 v29, v191, v191\n"
      "s_waitcnt lgkmcnt(8)\n"
      "v_mfma_f32_32x32x16_bf16 v[128:143], v[192:195], a[0:3], v[128:143]\n"
      "ds_read_b64_tr_b16 a[16:17], v26 offset:12288\n"
      "ds_read_b64_tr_b16 a[18:19], v26 offset:12544\n"
      "ds_read_b64_tr_b16 a[20:21], v26 offset:40960\n"
      "ds_read_b64_tr_b16 a[22:23], v26 offset:41216\n"
      "v_mfma_f32_16x16x32_bf16 v[34:37], v[30:33], a[0:3], v[34:37]\n"
      "s_waitcnt lgkmcnt(10)\n"
      "v_mfma_f32_32x32x16_bf16 v[112:127], v[192:195], a[4:7], v[112:127]\n"
      "s_add_u32 m0, s63, 8192\n"
      "ds_read_b128 v[204:207], v21 offset:0\n"
      "buffer_load_dwordx4 v3, s[8:11], s54 offen lds\n"
      "ds_read_b64_tr_b16 a[24:25], v27 offset:12288\n"
      "ds_read_b64_tr_b16 a[26:27], v27 offset:12544\n"
      "v_mfma_f32_16x16x32_bf16 v[38:41], v[30:33], a[4:7], v[38:41]\n"
      "s_waitcnt lgkmcnt(10)\n"
      "v_mfma_f32_32x32x16_bf16 v[128:143], v[196:199], a[8:11], v[128:143]\n"
      "ds_read_b64_tr_b16 a[28:29], v27 offset:40960\n"
      "ds_read_b64_tr_b16 a[30:31], v27 offset:41216\n"
      "s_waitcnt lgkmcnt(10)\n"
      "v_mfma_f32_32x32x16_bf16 v[112:127], v[196:199], a[12:15], v[112:127]\n"
      "s_add_u32 m0, s63, 9216\n"
      "s_nop 0\n"
      "buffer_load_dwordx4 v4, s[8:11], s54 offen lds\n"
      "s_waitcnt lgkmcnt(7)\n"
      "v_mfma_f32_32x32x16_bf16 v[128:143], v[200:203], a[16:19], v[128:143]\n"
      "s_waitcnt lgkmcnt(5)\n"
      "v_mfma_f32_32x32x16_bf16 v[112:127], v[200:203], a[20:23], v[112:127]\n"
      "s_add_u32 m0, s63, 10240\n"
      "s_nop 0\n"
      "buffer_load_dwordx4 v5, s[8:11], s54 offen lds\n"
      "s_waitcnt lgkmcnt(2)\n"
      "v_mfma_f32_32x32x16_bf16 v[128:143], v[204:207], a[24:27], v[128:143]\n"
      "s_waitcnt lgkmcnt(0)\n"
      "v_mfma_f32_32x32x16_bf16 v[112:127], v[204:207], a[28:31], v[112:127]\n"
      "s_add_u32 m0, s63, 11264\n"
      "s_nop 0\n"
      "buffer_load_dwordx4 v6, s[8:11], s54 offen lds\n"
      "s_waitcnt vmcnt(6)\n"
      "s_barrier\n"
      "ds_read_b128 v[208:211], v18 offset:4096\n"
      "ds_read_b64_tr_b16 a[0:1], v24 offset:16384\n"
      "ds_read_b64_tr_b16 a[2:3], v24 offset:16640\n"
      "ds_read_b64_tr_b16 a[4:5], v24 offset:45056\n"
      "ds_read_b64_tr_b16 a[6:7], v24 offset:45312\n"
      "s_add_u32 m0, s64, 24576\n"
      "ds_read_b128 v[212:215], v19 offset:4096\n"
      "buffer_load_dwordx4 v7, s[12:15], s62 offen lds\n"
      "ds_read_b64_tr_b16 a[8:9], v25 offset:16384\n"
      "ds_read_b64_tr_b16 a[10:11], v25 offset:16640\n"
      "ds_read_b64_tr_b16 a[12:13], v25 offset:45056\n"
      "ds_read_b64_tr_b16 a[14:15], v25 offset:45312\n"
      "v_dot2c_f32_bf16 v11, s67, v192\n"
      "v_dot2c_f32_bf16 v29, v192, v192\n"
      "v_dot2c_f32_bf16 v11, s67, v193\n"
      "v_dot2c_f32_bf16 v29, v193, v193\n"
      "v_dot2c_f32_bf16 v11, s67, v194\n"
      "v_dot2c_f32_bf16 v29, v194, v194\n"
      "v_dot2c_f32_bf16 v11, s67, v195\n"
      "v_dot2c_f32_bf16 v29, v195, v195\n"
      "v_dot2c_f32_bf16 v11, s67, v196\n"
      "v_dot2c_f32_bf16 v29, v196, v196\n"
      "v_dot2c_f32_bf16 v11, s67, v197\n"
      "v_dot2c_f32_bf16 v29, v197, v197\n"
      "v_dot2c_f32_bf16 v11, s67, v198\n"
      "v_dot2c_f32_bf16 v29, v198, v198\n"
      "v_dot2c_f32_bf16 v11, s67, v199\n"
      "v_dot2c_f32_bf16 v29, v199, v199\n"
      "s_add_u32 m0, s64, 53248\n"
      "ds_read_b128 v[216:219], v20 offset:4096\n"
      "buffer_load_dwordx4 v8, s[12:15], s62 offen lds\n"
      "v_dot2c_f32_bf16 v11, s67, v200\n"
      "v_dot2c_f32_bf16 v29, v200, v200\n"
      "v_dot2c_f32_bf16 v11, s67, v201\n"
      "v_dot2c_f32_bf16 v29, v201, v201\n"
      "v_dot2c_f32_bf16 v11, s67, v202\n"
      "v_dot2c_f32_bf16 v29, v202, v202\n"
      "v_dot2c_f32_bf16 v11, s67, v203\n"
      "v_dot2c_f32_bf16 v29, v203, v203\n"
      "v_dot2c_f32_bf16 v11, s67, v204\n"
      "v_dot2c_f32_bf16 v29, v204, v204\n"
      "v_dot2c_f32_bf16 v11, s67, v205\n"
      "v_dot2c_f32_bf16 v29, v205, v205\n"
      "v_dot2c_f32_bf16 v11, s67, v206\n"
      "v_dot2c_f32_bf16 v29, v206, v206\n"
      "v_dot2c_f32_bf16 v11, s67, v207\n"
      "v_dot2c_f32_bf16 v29, v207, v207\n"
      "s_waitcnt lgkmcnt(8)\n"
      "v_mfma_f32_32x32x16_bf16 v[128:143], v[208:211], a[0:3], v[128:143]\n"
      "ds_read_b64_tr_b16 a[16:17], v26 offset:16384\n"
      "ds_read_b64_tr_b16 a[18:19], v26 offset:16640\n"
      "ds_read_b64_tr_b16 a[20:21], v26 offset:45056\n"
      "ds_read_b64_tr_b16 a[22:23], v26 offset:45312\n"
      "v_mfma_f32_16x16x32_bf16 v[34:37], v[30:33], a[0:3], v[34:37]\n"
      "s_waitcnt lgkmcnt(10)\n"
      "v_mfma_f32_32x32x16_bf16 v[112:127], v[208:211], a[4:7], v[112:127]\n"
      "s_mov_b32 m0, s63\n"
      "ds_read_b128 v[220:223], v21 offset:4096\n"
      "buffer_load_dwordx4 v3, s[8:11], s55 offen lds\n"
      "ds_read_b64_tr_b16 a[24:25], v27 offset:16384\n"
      "ds_read_b64_tr_b16 a[26:27], v27 offset:16640\n"
      "v_mfma_f32_16x16x32_bf16 v[38:41], v[30:33], a[4:7], v[38:41]\n"
      "s_waitcnt lgkmcnt(10)\n"
      "v_mfma_f32_32x32x16_bf16 v[128:143], v[212:215], a[8:11], v[128:143]\n"
      "ds_read_b64_tr_b16 a[28:29], v27 offset:45056\n"
      "ds_read_b64_tr_b16 a[30:31], v27 offset:45312\n"
      "s_waitcnt lgkmcnt(10)\n"
      "v_mfma_f32_32x32x16_bf16 v[112:127], v[212:215], a[12:15], v[112:127]\n"
      "s_add_u32 m0, s63, 1024\n"
      "s_nop 0\n"
      "buffer_load_dwordx4 v4, s[8:11], s55 offen lds\n"
      "s_waitcnt lgkmcnt(7)\n"
      "v_mfma_f32_32x32x16_bf16 v[128:143], v[216:219], a[16:19], v[128:143]\n"
      "s_waitcnt lgkmcnt(5)\n"
      "v_mfma_f32_32x32x16_bf16 v[112:127], v[216:219], a[20:23], v[112:127]\n"
      "s_add_u32 m0, s63, 2048\n"
      "s_nop 0\n"
      "buffer_load_dwordx4 v5, s[8:11], s55 offen lds\n"
      "s_waitcnt lgkmcnt(2)\n"
      "v_mfma_f32_32x32x16_bf16 v[128:143], v[220:223], a[24:27], v[128:143]\n"
      "s_waitcnt lgkmcnt(0)\n"
      "v_mfma_f32_32x32x16_bf16 v[112:127], v[220:223], a[28:31], v[112:127]\n"
      "s_add_u32 m0, s63, 3072\n"
      "s_nop 0\n"
      "buffer_load_dwordx4 v6, s[8:11], s55 offen lds\n"
      "v_add_u32 v7, -896, v7\n"
      "s_waitcnt vmcnt(6)\n"
      "s_barrier\n"
      "ds_read_b128 v[224:227], v18 offset:8192\n"
      "ds_read_b64_tr_b16 a[0:1], v24 offset:20480\n"
      "ds_read_b64_tr_b16 a[2:3], v24 offset:20736\n"
      "ds_read_b64_tr_b16 a[4:5], v24 offset:49152\n"
      "ds_read_b64_tr_b16 a[6:7], v24 offset:49408\n"
      "s_add_u32 m0, s64, 106496\n"
      "ds_read_b128 v[228:231], v19 offset:8192\n"
      "buffer_load_dwordx4 v7, s[12:15], s56 offen lds\n"
      "ds_read_b64_tr_b16 a[8:9], v25 offset:20480\n"
      "ds_read_b64_tr_b16 a[10:11], v25 offset:20736\n"
      "ds_read_b64_tr_b16 a[12:13], v25 offset:49152\n"
      "ds_read_b64_tr_b16 a[14:15], v25 offset:49408\n"
      "v_dot2c_f32_bf16 v11, s67, v208\n"
      "v_dot2c_f32_bf16 v29, v208, v208\n"
      "v_dot2c_f32_bf16 v11, s67, v209\n"
      "v_dot2c_f32_bf16 v29, v209, v209\n"
      "v_dot2c_f32_bf16 v11, s67, v210\n"
      "v_dot2c_f32_bf16 v29, v210, v210\n"
      "v_dot2c_f32_bf16 v11, s67, v211\n"
      "v_dot2c_f32_bf16 v29, v211, v211\n"
      "v_dot2c_f32_bf16 v11, s67, v212\n"
      "v_dot2c_f32_bf16 v29, v212, v212\n"
      "v_dot2c_f32_bf16 v11, s67, v213\n"
      "v_dot2c_f32_bf16 v29, v213, v213\n"
      "v_dot2c_f32_bf16 v11, s67, v214\n"
      "v_dot2c_f32_bf16 v29, v214, v214\n"
      "v_dot2c_f32_bf16 v11, s67, v215\n"
      "v_dot2c_f32_bf16 v29, v215, v215\n"
      "v_dot2c_f32_bf16 v11, s67, v216\n"
      "v_dot2c_f32_bf16 v29, v216, v216\n"
      "v_dot2c_f32_bf16 v11, s67, v217\n"
      "v_dot2c_f32_bf16 v29, v217, v217\n"
      "v_dot2c_f32_bf16 v11, s67, v218\n"
      "v_dot2c_f32_bf16 v29, v218, v218\n"
      "v_dot2c_f32_bf16 v11, s67, v219\n"
      "v_dot2c_f32_bf16 v29, v219, v219\n"
      "v_dot2c_f32_bf16 v11, s67, v220\n"
      "v_dot2c_f32_bf16 v29, v220, v220\n"
      "v_dot2c_f32_bf16 v11, s67, v221\n"
      "v_dot2c_f32_bf16 v29, v221, v221\n"
      "v_dot2c_f32_bf16 v11, s67, v222\n"
      "v_dot2c_f32_bf16 v29, v222, v222\n"
      "v_dot2c_f32_bf16 v11, s67, v223\n"
      "v_dot2c_f32_bf16 v29, v223, v223\n"
      "s_waitcnt lgkmcnt(7)\n"
      "v_mfma_f32_32x32x16_bf16 v[128:143], v[224:227], a[0:3], v[128:143]\n"
      "ds_read_b128 v[232:235], v20 offset:8192\n"
      "ds_read_b64_tr_b16 a[16:17], v26 offset:20480\n"
      "ds_read_b64_tr_b16 a[18:19], v26 offset:20736\n"
      "ds_read_b64_tr_b16 a[20:21], v26 offset:49152\n"
      "v_mfma_f32_16x16x32_bf16 v[34:37], v[30:33], a[0:3], v[34:37]\n"
      "s_waitcnt lgkmcnt(9)\n"
      "v_mfma_f32_32x32x16_bf16 v[112:127], v[224:227], a[4:7], v[112:127]\n"
      "s_add_u32 m0, s64, 110592\n"
      "ds_read_b64_tr_b16 a[22:23], v26 offset:49408\n"
      "buffer_load_dwordx4 v7, s[12:15], s57 offen lds\n"
      "ds_read_b128 v[236:239], v21 offset:8192\n"
      "ds_read_b64_tr_b16 a[24:25], v27 offset:20480\n"
      "v_mfma_f32_16x16x32_bf16 v[38:41], v[30:33], a[4:7], v[38:41]\n"
      "s_waitcnt lgkmcnt(9)\n"
      "v_mfma_f32_32x32x16_bf16 v[128:143], v[228:231], a[8:11], v[128:143]\n"
      "ds_read_b64_tr_b16 a[26:27], v27 offset:20736\n"
      "ds_read_b64_tr_b16 a[28:29], v27 offset:49152\n"
      "ds_read_b64_tr_b16 a[30:31], v27 offset:49408\n"
      "s_waitcnt lgkmcnt(10)\n"
      "v_mfma_f32_32x32x16_bf16 v[112:127], v[228:231], a[12:15], v[112:127]\n"
      "s_waitcnt lgkmcnt(7)\n"
      "v_mfma_f32_32x32x16_bf16 v[128:143], v[232:235], a[16:19], v[128:143]\n"
      "s_waitcnt lgkmcnt(5)\n"
      "v_mfma_f32_32x32x16_bf16 v[112:127], v[232:235], a[20:23], v[112:127]\n"
      "s_waitcnt lgkmcnt(2)\n"
      "v_mfma_f32_32x32x16_bf16 v[128:143], v[236:239], a[24:27], v[128:143]\n"
      "s_waitcnt lgkmcnt(0)\n"
      "v_mfma_f32_32x32x16_bf16 v[112:127], v[236:239], a[28:31], v[112:127]\n"
      "s_waitcnt vmcnt(2)\n"
      "s_barrier\n"
      "ds_read_b128 v[240:243], v18 offset:0\n"
      "ds_read_b128 v[244:247], v19 offset:0\n"
      "ds_read_b128 v[248:251], v20 offset:0\n"
      "ds_read_b128 v[252:255], v21 offset:0\n"
      "ds_read_b64_tr_b16 a[0:1], v24 offset:24576\n"
      "ds_read_b64_tr_b16 a[2:3], v24 offset:24832\n"
      "ds_read_b64_tr_b16 a[4:5], v24 offset:53248\n"
      "ds_read_b64_tr_b16 a[6:7], v24 offset:53504\n"
      "ds_read_b64_tr_b16 a[8:9], v25 offset:24576\n"
      "ds_read_b64_tr_b16 a[10:11], v25 offset:24832\n"
      "ds_read_b64_tr_b16 a[16:17], v26 offset:24576\n"
      "ds_read_b64_tr_b16 a[18:19], v26 offset:24832\n"
      "ds_read_b64_tr_b16 a[24:25], v27 offset:24576\n"
      "ds_read_b64_tr_b16 a[26:27], v27 offset:24832\n"
      "v_dot2c_f32_bf16 v11, s67, v224\n"
      "v_dot2c_f32_bf16 v29, v224, v224\n"
      "v_dot2c_f32_bf16 v11, s67, v225\n"
      "v_dot2c_f32_bf16 v29, v225, v225\n"
      "v_dot2c_f32_bf16 v11, s67, v226\n"
      "v_dot2c_f32_bf16 v29, v226, v226\n"
      "v_dot2c_f32_bf16 v11, s67, v227\n"
      "v_dot2c_f32_bf16 v29, v227, v227\n"
      "v_dot2c_f32_bf16 v11, s67, v228\n"
      "v_dot2c_f32_bf16 v29, v228, v228\n"
      "v_dot2c_f32_bf16 v11, s67, v229\n"
      "v_dot2c_f32_bf16 v29, v229, v229\n"
      "v_dot2c_f32_bf16 v11, s67, v230\n"
      "v_dot2c_f32_bf16 v29, v230, v230\n"
      "v_dot2c_f32_bf16 v11, s67, v231\n"
      "v_dot2c_f32_bf16 v29, v231, v231\n"
      "v_dot2c_f32_bf16 v11, s67, v232\n"
      "v_dot2c_f32_bf16 v29, v232, v232\n"
      "v_dot2c_f32_bf16 v11, s67, v233\n"
      "v_dot2c_f32_bf16 v29, v233, v233\n"
      "v_dot2c_f32_bf16 v11, s67, v234\n"
      "v_dot2c_f32_bf16 v29, v234, v234\n"
      "v_dot2c_f32_bf16 v11, s67, v235\n"
      "v_dot2c_f32_bf16 v29, v235, v235\n"
      "v_dot2c_f32_bf16 v11, s67, v236\n"
      "v_dot2c_f32_bf16 v29, v236, v236\n"
      "v_dot2c_f32_bf16 v11, s67, v237\n"
      "v_dot2c_f32_bf16 v29, v237, v237\n"
      "v_dot2c_f32_bf16 v11, s67, v238\n"
      "v_dot2c_f32_bf16 v29, v238, v238\n"
      "v_dot2c_f32_bf16 v11, s67, v239\n"
      "v_dot2c_f32_bf16 v29, v239, v239\n"
      "ds_read_b64_tr_b16 a[12:13], v25 offset:53248\n"
      "ds_read_b64_tr_b16 a[14:15], v25 offset:53504\n"
      "ds_read_b64_tr_b16 a[20:21], v26 offset:53248\n"
      "ds_read_b64_tr_b16 a[22:23], v26 offset:53504\n"
      "ds_read_b64_tr_b16 a[28:29], v27 offset:53248\n"
      "ds_read_b64_tr_b16 a[30:31], v27 offset:53504\n"
      "s_waitcnt lgkmcnt(14)\n"
      "v_dot2c_f32_bf16 v11, s67, v240\n"
      "v_dot2c_f32_bf16 v29, v240, v240\n"
      "v_dot2c_f32_bf16 v11, s67, v241\n"
      "v_dot2c_f32_bf16 v29, v241, v241\n"
      "v_dot2c_f32_bf16 v11, s67, v242\n"
      "v_dot2c_f32_bf16 v29, v242, v242\n"
      "v_dot2c_f32_bf16 v11, s67, v243\n"
      "v_dot2c_f32_bf16 v29, v243, v243\n"
      "v_dot2c_f32_bf16 v11, s67, v244\n"
      "v_dot2c_f32_bf16 v29, v244, v244\n"
      "v_dot2c_f32_bf16 v11, s67, v245\n"
      "v_dot2c_f32_bf16 v29, v245, v245\n"
      "v_dot2c_f32_bf16 v11, s67, v246\n"
      "v_dot2c_f32_bf16 v29, v246, v246\n"
      "v_dot2c_f32_bf16 v11, s67, v247\n"
      "v_dot2c_f32_bf16 v29, v247, v247\n"
      "v_dot2c_f32_bf16 v11, s67, v248\n"
      "v_dot2c_f32_bf16 v29, v248, v248\n"
      "v_dot2c_f32_bf16 v11, s67, v249\n"
      "v_dot2c_f32_bf16 v29, v249, v249\n"
      "v_dot2c_f32_bf16 v11, s67, v250\n"
      "v_dot2c_f32_bf16 v29, v250, v250\n"
      "v_dot2c_f32_bf16 v11, s67, v251\n"
      "v_dot2c_f32_bf16 v29, v251, v251\n"
      "v_dot2c_f32_bf16 v11, s67, v252\n"
      "v_dot2c_f32_bf16 v29, v252, v252\n"
      "v_dot2c_f32_bf16 v11, s67, v253\n"
      "v_dot2c_f32_bf16 v29, v253, v253\n"
      "v_dot2c_f32_bf16 v11, s67, v254\n"
      "v_dot2c_f32_bf16 v29, v254, v254\n"
      "v_dot2c_f32_bf16 v11, s67, v255\n"
      "v_dot2c_f32_bf16 v29, v255, v255\n"
      "v_mfma_f32_16x16x32_bf16 v[34:37], v[30:33], a[0:3], v[34:37]\n"
      "s_nop 0\n"
      "v_mov_b32 v50, v11\n"
      "v_mov_b32 v51, v29\n"
      "s_waitcnt lgkmcnt(12)\n"
      "v_mfma_f32_16x16x32_bf16 v[38:41], v[30:33], a[4:7], v[38:41]\n"
      "v_readfirstlane_b32 s73, v15\n"
      "v_permlane32_swap_b32 v11, v50\n"
      "v_mfma_f32_32x32x16_bf16 v[128:143], v[240:243], a[0:3], v[128:143]\n"
      "v_permlane32_swap_b32 v29, v51\n"
      "v_add_f32 v11, v11, v50\n"
      "v_add_f32 v29, v29, v51\n"
      "v_mul_f32 v11, 0xbb124925, v11\n"
      "v_mul_f32 v29, 0x3b124925, v29\n"
      "s_waitcnt lgkmcnt(10)\n"
      "v_mfma_f32_32x32x16_bf16 v[128:143], v[244:247], a[8:11], v[128:143]\n"
      "v_cndmask_b32 v51, v34, v38, s[74:75]\n"
      "ds_write_b32 v46, v51\n"
      "v_fma_f32 v29, -v11, v11, v29\n"
      "v_max_f32 v29, 0, v29\n"
      "v_add_f32 v29, 0x3727c5ac, v29\n"
      "s_waitcnt lgkmcnt(9)\n"
      "v_mfma_f32_32x32x16_bf16 v[128:143], v[248:251], a[16:19], v[128:143]\n"
      "v_rsq_f32 v29, v29\n"
      "s_mul_i32 s73, s73, 0x9E3779B1\n"
      "v_mul_f32 v48, 0x3fb8aa3b, v29\n"
      "v_mul_f32 v49, v11, v48\n"
      "v_cndmask_b32 v50, v48, v49, s[74:75]\n"
      "s_waitcnt lgkmcnt(7)\n"
      "v_mfma_f32_32x32x16_bf16 v[128:143], v[252:255], a[24:27], v[128:143]\n"
      "ds_write_b32 v44, v50\n"
      "s_waitcnt lgkmcnt(1)\n"
      "s_barrier\n"
      "ds_read_b128 v[34:37], v47\n"
      "ds_read_b128 v[38:41], v47 offset:512\n"
      "ds_read_b128 v[72:75], v45 offset:128\n"
      "v_mfma_f32_32x32x16_bf16 v[112:127], v[240:243], a[4:7], v[112:127]\n"
      "ds_read_b128 v[88:91], v45 offset:0\n"
      "ds_read_b128 v[76:79], v45 offset:160\n"
      "ds_read_b128 v[92:95], v45 offset:32\n"
      "ds_read_b128 v[80:83], v45 offset:192\n"
      "ds_read_b128 v[96:99], v45 offset:64\n"
      "v_mfma_f32_32x32x16_bf16 v[112:127], v[244:247], a[12:15], v[112:127]\n"
      "ds_read_b128 v[84:87], v45 offset:224\n"
      "ds_read_b128 v[100:103], v45 offset:96\n"
      "v_mfma_f32_32x32x16_bf16 v[112:127], v[248:251], a[20:23], v[112:127]\n"
      "s_add_u32 m0, s64, 114688\n"
      "s_nop 0\n"
      "buffer_load_dwordx4 v7, s[12:15], s58 offen lds\n"
      "v_mfma_f32_32x32x16_bf16 v[112:127], v[252:255], a[28:31], v[112:127]\n"
      "s_add_u32 m0, s64, 118784\n"
      "s_nop 0\n"
      "buffer_load_dwordx4 v7, s[12:15], s59 offen lds\n"
      "s_add_u32 s49, s73, 0x7F4A7C15\n"
      "s_and_b32 s73, s49, 0xBFFFBFFF\n"
      "s_waitcnt lgkmcnt(9)\n"
      "v_pk_add_f32 v[34:35], v[34:35], v[36:37]\n"
      "s_waitcnt lgkmcnt(8)\n"
      "v_pk_add_f32 v[38:39], v[38:39], v[40:41]\n"
      "v_add_f32 v34, v34, v35\n"
      "v_add_f32 v35, v38, v39\n"
      "s_waitcnt lgkmcnt(7)\n"
      "v_pk_fma_f32 v[56:57], v[72:73], v[34:35], v[42:43] op_sel:[0,0,0] op_sel_hi:[1,0,0]\n"
      "s_waitcnt lgkmcnt(6)\n"
      "v_pk_fma_f32 v[128:129], v[88:89], v[128:129], v[56:57]\n"
      "v_pk_fma_f32 v[58:59], v[74:75], v[34:35], v[42:43] op_sel:[0,0,0] op_sel_hi:[1,0,0]\n"
      "v_pk_fma_f32 v[130:131], v[90:91], v[130:131], v[58:59]\n"
      "s_waitcnt lgkmcnt(5)\n"
      "v_pk_fma_f32 v[60:61], v[76:77], v[34:35], v[42:43] op_sel:[0,0,0] op_sel_hi:[1,0,0]\n"
      "s_waitcnt lgkmcnt(4)\n"
      "v_pk_fma_f32 v[132:133], v[92:93], v[132:133], v[60:61]\n"
      "v_pk_fma_f32 v[62:63], v[78:79], v[34:35], v[42:43] op_sel:[0,0,0] op_sel_hi:[1,0,0]\n"
      "v_pk_fma_f32 v[134:135], v[94:95], v[134:135], v[62:63]\n"
      "s_waitcnt lgkmcnt(3)\n"
      "v_pk_fma_f32 v[64:65], v[80:81], v[34:35], v[42:43] op_sel:[0,0,0] op_sel_hi:[1,0,0]\n"
      "s_waitcnt lgkmcnt(2)\n"
      "v_pk_fma_f32 v[136:137], v[96:97], v[136:137], v[64:65]\n"
      "v_pk_fma_f32 v[66:67], v[82:83], v[34:35], v[42:43] op_sel:[0,0,0] op_sel_hi:[1,0,0]\n"
      "v_pk_fma_f32 v[138:139], v[98:99], v[138:139], v[66:67]\n"
      "s_waitcnt lgkmcnt(1)\n"
      "v_pk_fma_f32 v[68:69], v[84:85], v[34:35], v[42:43] op_sel:[0,0,0] op_sel_hi:[1,0,0]\n"
      "s_waitcnt lgkmcnt(0)\n"
      "v_pk_fma_f32 v[140:141], v[100:101], v[140:141], v[68:69]\n"
      "v_pk_fma_f32 v[70:71], v[86:87], v[34:35], v[42:43] op_sel:[0,0,0] op_sel_hi:[1,0,0]\n"
      "v_pk_fma_f32 v[142:143], v[102:103], v[142:143], v[70:71]\n"
      "v_pk_mul_f32 v[72:73], v[72:73], v[34:35] op_sel:[0,1] op_sel_hi:[1,1]\n"
      "v_pk_fma_f32 v[112:113], v[88:89], v[112:113], v[72:73]\n"
      "v_pk_mul_f32 v[74:75], v[74:75], v[34:35] op_sel:[0,1] op_sel_hi:[1,1]\n"
      "v_pk_fma_f32 v[114:115], v[90:91], v[114:115], v[74:75]\n"
      "v_pk_mul_f32 v[76:77], v[76:77], v[34:35] op_sel:[0,1] op_sel_hi:[1,1]\n"
      "v_pk_fma_f32 v[116:117], v[92:93], v[116:117], v[76:77]\n"
      "v_pk_mul_f32 v[78:79], v[78:79], v[34:35] op_sel:[0,1] op_sel_hi:[1,1]\n"
      "v_pk_fma_f32 v[118:119], v[94:95], v[118:119], v[78:79]\n"
      "v_pk_mul_f32 v[80:81], v[80:81], v[34:35] op_sel:[0,1] op_sel_hi:[1,1]\n"
      "v_pk_fma_f32 v[120:121], v[96:97], v[120:121], v[80:81]\n"
      "v_pk_mul_f32 v[82:83], v[82:83], v[34:35] op_sel:[0,1] op_sel_hi:[1,1]\n"
      "v_pk_fma_f32 v[122:123], v[98:99], v[122:123], v[82:83]\n"
      "v_pk_mul_f32 v[84:85], v[84:85], v[34:35] op_sel:[0,1] op_sel_hi:[1,1]\n"
      "v_pk_fma_f32 v[124:125], v[100:101], v[124:125], v[84:85]\n"
      "v_pk_mul_f32 v[86:87], v[86:87], v[34:35] op_sel:[0,1] op_sel_hi:[1,1]\n"
      "v_pk_fma_f32 v[126:127], v[102:103], v[126:127], v[86:87]\n"
      "s_mov_b32 exec_lo, 0x0fffffff\n"
      "s_mov_b32 exec_hi, 0x0fffffff\n"
      "v_cvt_pk_bf16_f32 v104, v112, v113\n"
      "v_cvt_pk_bf16_f32 v105, v114, v115\n"
      "v_cvt_pk_bf16_f32 v106, v116, v117\n"
      "v_cvt_pk_bf16_f32 v107, v118, v119\n"
      "v_cvt_pk_bf16_f32 v108, v120, v121\n"
      "v_cvt_pk_bf16_f32 v109, v122, v123\n"
      "v_cvt_pk_bf16_f32 v110, v124, v125\n"
      "v_cvt_pk_bf16_f32 v111, v126, v127\n"
      "s_mov_b64 exec, -1\n"
      "v_exp_f32_e64 v56, v128 clamp\n"
      "v_max_f32 v128, v128, v42\n"
      "s_add_u32 m0, s64, 122880\n"
      "s_nop 0\n"
      "buffer_load_dwordx4 v7, s[12:15], s60 offen lds\n"
      "v_exp_f32_e64 v57, v129 clamp\n"
      "v_max_f32 v129, v129, v42\n"
      "v_exp_f32_e64 v58, v130 clamp\n"
      "v_max_f32 v130, v130, v42\n"
      "v_exp_f32_e64 v59, v131 clamp\n"
      "v_max_f32 v131, v131, v42\n"
      "v_exp_f32_e64 v60, v132 clamp\n"
      "v_max_f32 v132, v132, v42\n"
      "v_exp_f32_e64 v61, v133 clamp\n"
      "v_max_f32 v133, v133, v42\n"
      "v_exp_f32_e64 v62, v134 clamp\n"
      "v_max_f32 v134, v134, v42\n"
      "v_exp_f32_e64 v63, v135 clamp\n"
      "v_max_f32 v135, v135, v42\n"
      "v_exp_f32_e64 v64, v136 clamp\n"
      "v_max_f32 v136, v136, v42\n"
      "v_exp_f32_e64 v65, v137 clamp\n"
      "v_max_f32 v137, v137, v42\n"
      "v_exp_f32_e64 v66, v138 clamp\n"
      "v_max_f32 v138, v138, v42\n"
      "v_exp_f32_e64 v67, v139 clamp\n"
      "v_max_f32 v139, v139, v42\n"
      "v_exp_f32_e64 v68, v140 clamp\n"
      "v_max_f32 v140, v140, v42\n"
      "v_exp_f32_e64 v69, v141 clamp\n"
      "v_max_f32 v141, v141, v42\n"
      "v_exp_f32_e64 v70, v142 clamp\n"
      "v_max_f32 v142, v142, v42\n"
      "v_exp_f32_e64 v71, v143 clamp\n"
      "v_max_f32 v143, v143, v42\n"
      "s_add_u32 m0, s64, 126976\n"
      "s_nop 0\n"
      "buffer_load_dwordx4 v7, s[12:15], s61 offen lds\n"
      "v_pk_fma_f32 v[128:129], v[128:129], v[42:43], v[56:57] op_sel:[0,1,0] op_sel_hi:[1,1,1]\n"
      "v_pk_fma_f32 v[130:131], v[130:131], v[42:43], v[58:59] op_sel:[0,1,0] op_sel_hi:[1,1,1]\n"
      "v_pk_fma_f32 v[132:133], v[132:133], v[42:43], v[60:61] op_sel:[0,1,0] op_sel_hi:[1,1,1]\n"
      "v_pk_fma_f32 v[134:135], v[134:135], v[42:43], v[62:63] op_sel:[0,1,0] op_sel_hi:[1,1,1]\n"
      "v_pk_fma_f32 v[136:137], v[136:137], v[42:43], v[64:65] op_sel:[0,1,0] op_sel_hi:[1,1,1]\n"
      "v_pk_fma_f32 v[138:139], v[138:139], v[42:43], v[66:67] op_sel:[0,1,0] op_sel_hi:[1,1,1]\n"
      "v_pk_fma_f32 v[140:141], v[140:141], v[42:43], v[68:69] op_sel:[0,1,0] op_sel_hi:[1,1,1]\n"
      "v_pk_fma_f32 v[142:143], v[142:143], v[42:43], v[70:71] op_sel:[0,1,0] op_sel_hi:[1,1,1]\n"
      "v_cvt_pk_bf16_f32 v72, v128, v129\n"
      "v_cvt_pk_bf16_f32 v73, v130, v131\n"
      "v_cvt_pk_bf16_f32 v74, v132, v133\n"
      "v_cvt_pk_bf16_f32 v75, v134, v135\n"
      "v_cvt_pk_bf16_f32 v76, v136, v137\n"
      "v_cvt_pk_bf16_f32 v77, v138, v139\n"
      "v_cvt_pk_bf16_f32 v78, v140, v141\n"
      "v_cvt_pk_bf16_f32 v79, v142, v143\n"
      "v_mfma_f32_32x32x16_bf16 v[56:71], v[72:75], v[104:107], 0\n"
      "s_nop 0\n"
      "v_mfma_f32_32x32x16_bf16 v[56:71], v[76:79], v[108:111], v[56:71]\n"
      "v_add_u32 v3, s63, v28\n"
      "s_add_u32 s46, s64, 57344\n"
      "v_add_u32 v4, s46, v28\n"
      "s_lshl_b32 s46, s45, 16\n"
      "s_add_u32 s46, s46, s64\n"
      "s_add_u32 s46, s46, 524288\n"
      "v_add_u32 v5, s46, v28\n"
      "s_lshl_b32 s48, s44, 12\n"
      "v_mov_b32 v18, s73\n"
      "v_mov_b32 v20, s73\n"
      "s_add_u32 m0, s64, 131072\n"
      "s_nop 0\n"
      "buffer_load_dwordx4 v7, s[12:15], s62 offen lds\n"
      "ds_write_b128 v3, v[56:59] offset:0\n"
      "ds_write_b128 v3, v[60:63] offset:1024\n"
      "ds_write_b128 v3, v[64:67] offset:2048\n"
      "ds_write_b128 v3, v[68:71] offset:3072\n"
      "v_add_u32 v24, 106496, v24\n"
      "v_add_u32 v25, 106496, v25\n"
      "v_add_u32 v26, 106496, v26\n"
      "v_add_u32 v27, 106496, v27\n"
      "v_and_b32 v2, 7, v1\n"
      "v_lshlrev_b32 v6, 1, v10\n"
      "v_xor_b32 v2, v2, v6\n"
      "v_lshlrev_b32 v2, 4, v2\n"
      "v_mul_u32_u24 v6, 896, v9\n"
      "s_xor_b32 s46, s33, s72\n"
      "s_mul_i32 s50, s46, 14336\n"
      "s_mul_i32 s46, s50, 7\n"
      "s_lshl_b32 s47, s71, 7\n"
      "s_add_u32 s46, s46, s47\n"
      "v_add3_u32 v6, v6, v2, s46\n"
      "s_lshl2_add_u32 s51, s70, s33\n"
      "s_mul_i32 s46, s51, 14336\n"
      "s_add_u32 s46, s46, s47\n"
      "v_add_u32 v8, s46, v13\n"
      "v_xor_b32 v14, 64, v8\n"
      "v_add_u32 v14, 7168, v14\n"
      "s_lshl_b32 s52, s33, 11\n"
      "s_waitcnt lgkmcnt(0)\n"
      "s_barrier\n"
      "ds_read_b128 v[88:91], v4 offset:0\n"
      "ds_read_b128 v[92:95], v4 offset:12288\n"
      "ds_read_b128 v[96:99], v4 offset:24576\n"
      "ds_read_b128 v[100:103], v4 offset:36864\n"
      "v_cvt_pk_bf16_f32 v38, v11, v11\n"
      "v_mov_b32 v40, 0\n"
      "v_lshlrev_b32 v2, 16, v38\n"
      "v_sub_f32 v2, v11, v2\n"
      "v_cvt_pk_bf16_f32 v39, v2, 0\n"
      "v_mov_b32 v41, 0\n"
      "v_mov_b32 v54, 0\n"
      "v_mov_b32 v55, 0\n"
      "s_waitcnt lgkmcnt(2)\n"
      "v_pk_add_f32 v[88:89], v[88:89], v[92:93]\n"
      "v_pk_add_f32 v[90:91], v[90:91], v[94:95]\n"
      "s_waitcnt lgkmcnt(0)\n"
      "v_pk_add_f32 v[96:97], v[96:97], v[100:101]\n"
      "v_pk_add_f32 v[98:99], v[98:99], v[102:103]\n"
      "v_pk_add_f32 v[88:89], v[88:89], v[96:97]\n"
      "v_pk_add_f32 v[90:91], v[90:91], v[98:99]\n"
      "v_cvt_pk_bf16_f32 v19, v88, v89\n"
      "v_cvt_pk_bf16_f32 v21, v90, v91\n"
      "buffer_store_dwordx4 v[18:21], v5, s[36:39], s48 offen sc1\n"
      "s_waitcnt vmcnt(1)\n"
      "s_barrier\n"
      "ds_read_b64_tr_b16 a[0:1], v24 offset:0\n"
      "ds_read_b64_tr_b16 a[2:3], v24 offset:256\n"
      "ds_read_b64_tr_b16 a[4:5], v24 offset:4096\n"
      "ds_read_b64_tr_b16 a[6:7], v24 offset:4352\n"
      "ds_read_b64_tr_b16 a[8:9], v24 offset:8192\n"
      "ds_read_b64_tr_b16 a[10:11], v24 offset:8448\n"
      "ds_read_b64_tr_b16 a[12:13], v24 offset:12288\n"
      "ds_read_b64_tr_b16 a[14:15], v24 offset:12544\n"
      "s_mov_b32 m0, s50\n"
      "s_nop 0\n"
      "buffer_load_dwordx4 v6, s[16:19], 0 offen lds\n"
      "s_add_u32 m0, s50, 1024\n"
      "s_mov_b32 s66, 7168\n"
      "buffer_load_dwordx4 v6, s[16:19], s66 offen lds\n"
      "s_waitcnt lgkmcnt(6)\n"
      "v_mfma_f32_16x16x32_bf16 v[34:37], v[30:33], a[0:3], 0\n"
      "v_mfma_f32_32x32x16_bf16 v[128:143], a[0:3], v[144:147], 0\n"
      "ds_read_b64_tr_b16 a[16:17], v24 offset:16384\n"
      "ds_read_b64_tr_b16 a[18:19], v24 offset:16640\n"
      "s_add_u32 m0, s50, 2048\n"
      "s_mov_b32 s66, 14336\n"
      "buffer_load_dwordx4 v6, s[16:19], s66 offen lds\n"
      "s_waitcnt lgkmcnt(6)\n"
      "v_mfma_f32_16x16x32_bf16 v[34:37], v[30:33], a[4:7], v[34:37]\n"
      "v_mfma_f32_32x32x16_bf16 v[128:143], a[4:7], v[160:163], v[128:143]\n"
      "ds_read_b64_tr_b16 a[0:1], v24 offset:20480\n"
      "ds_read_b64_tr_b16 a[2:3], v24 offset:20736\n"
      "s_add_u32 m0, s50, 3072\n"
      "s_mov_b32 s66, 21504\n"
      "buffer_load_dwordx4 v6, s[16:19], s66 offen lds\n"
      "s_waitcnt lgkmcnt(6)\n"
      "v_mfma_f32_16x16x32_bf16 v[34:37], v[30:33], a[8:11], v[34:37]\n"
      "v_mfma_f32_32x32x16_bf16 v[128:143], a[8:11], v[176:179], v[128:143]\n"
      "ds_read_b64_tr_b16 a[4:5], v24 offset:24576\n"
      "ds_read_b64_tr_b16 a[6:7], v24 offset:24832\n"
      "s_add_u32 m0, s50, 4096\n"
      "s_mov_b32 s66, 28672\n"
      "buffer_load_dwordx4 v6, s[16:19], s66 offen lds\n"
      "s_waitcnt lgkmcnt(6)\n"
      "v_mfma_f32_16x16x32_bf16 v[34:37], v[30:33], a[12:15], v[34:37]\n"
      "v_mfma_f32_32x32x16_bf16 v[128:143], a[12:15], v[192:195], v[128:143]\n"
      "ds_read_b64_tr_b16 a[8:9], v25 offset:0\n"
      "ds_read_b64_tr_b16 a[10:11], v25 offset:256\n"
      "s_add_u32 m0, s50, 5120\n"
      "s_mov_b32 s66, 35840\n"
      "buffer_load_dwordx4 v6, s[16:19], s66 offen lds\n"
      "s_waitcnt lgkmcnt(6)\n"
      "v_mfma_f32_16x16x32_bf16 v[34:37], v[30:33], a[16:19], v[34:37]\n"
      "v_mfma_f32_32x32x16_bf16 v[128:143], a[16:19], v[208:211], v[128:143]\n"
      "ds_read_b64_tr_b16 a[12:13], v26 offset:0\n"
      "ds_read_b64_tr_b16 a[14:15], v26 offset:256\n"
      "s_add_u32 m0, s50, 6144\n"
      "s_mov_b32 s66, 43008\n"
      "buffer_load_dwordx4 v6, s[16:19], s66 offen lds\n"
      "s_waitcnt lgkmcnt(6)\n"
      "v_mfma_f32_16x16x32_bf16 v[34:37], v[30:33], a[0:3], v[34:37]\n"
      "v_mfma_f32_32x32x16_bf16 v[128:143], a[0:3], v[224:227], v[128:143]\n"
      "ds_read_b64_tr_b16 a[16:17], v27 offset:0\n"
      "ds_read_b64_tr_b16 a[18:19], v27 offset:256\n"
      "s_add_u32 m0, s50, 7168\n"
      "s_mov_b32 s66, 50176\n"
      "buffer_load_dwordx4 v6, s[16:19], s66 offen lds\n"
      "s_waitcnt lgkmcnt(6)\n"
      "v_mfma_f32_16x16x32_bf16 v[34:37], v[30:33], a[4:7], v[34:37]\n"
      "v_mfma_f32_32x32x16_bf16 v[128:143], a[4:7], v[240:243], v[128:143]\n"
      "ds_read_b64_tr_b16 a[0:1], v25 offset:4096\n"
      "ds_read_b64_tr_b16 a[2:3], v25 offset:4352\n"
      "s_add_u32 m0, s50, 8192\n"
      "s_mov_b32 s66, 57344\n"
      "buffer_load_dwordx4 v6, s[16:19], s66 offen lds\n"
      "s_waitcnt lgkmcnt(6)\n"
      "v_mfma_f32_32x32x16_bf16 v[128:143], a[8:11], v[148:151], v[128:143]\n"
      "ds_read_b64_tr_b16 a[4:5], v26 offset:4096\n"
      "ds_read_b64_tr_b16 a[6:7], v26 offset:4352\n"
      "s_add_u32 m0, s50, 9216\n"
      "s_mov_b32 s66, 64512\n"
      "buffer_load_dwordx4 v6, s[16:19], s66 offen lds\n"
      "s_waitcnt lgkmcnt(6)\n"
      "v_mfma_f32_32x32x16_bf16 v[128:143], a[12:15], v[152:155], v[128:143]\n"
      "ds_read_b64_tr_b16 a[8:9], v27 offset:4096\n"
      "ds_read_b64_tr_b16 a[10:11], v27 offset:4352\n"
      "ds_write_b32 v46, v34\n"
      "s_waitcnt lgkmcnt(7)\n"
      "v_mfma_f32_32x32x16_bf16 v[128:143], a[16:19], v[156:159], v[128:143]\n"
      "ds_read_b64_tr_b16 a[12:13], v25 offset:8192\n"
      "ds_read_b64_tr_b16 a[14:15], v25 offset:8448\n"
      "s_add_u32 m0, s50, 10240\n"
      "s_mov_b32 s66, 71680\n"
      "buffer_load_dwordx4 v6, s[16:19], s66 offen lds\n"
      "s_waitcnt lgkmcnt(7)\n"
      "v_mfma_f32_32x32x16_bf16 v[128:143], a[0:3], v[164:167], v[128:143]\n"
      "ds_read_b64_tr_b16 a[16:17], v26 offset:8192\n"
      "ds_read_b64_tr_b16 a[18:19], v26 offset:8448\n"
      "s_waitcnt lgkmcnt(7)\n"
      "v_mfma_f32_32x32x16_bf16 v[128:143], a[4:7], v[168:171], v[128:143]\n"
      "ds_read_b64_tr_b16 a[0:1], v27 offset:8192\n"
      "ds_read_b64_tr_b16 a[2:3], v27 offset:8448\n"
      "s_add_u32 m0, s50, 11264\n"
      "s_mov_b32 s66, 78848\n"
      "buffer_load_dwordx4 v6, s[16:19], s66 offen lds\n"
      "s_waitcnt lgkmcnt(7)\n"
      "v_mfma_f32_32x32x16_bf16 v[128:143], a[8:11], v[172:175], v[128:143]\n"
      "ds_read_b64_tr_b16 a[4:5], v25 offset:12288\n"
      "ds_read_b64_tr_b16 a[6:7], v25 offset:12544\n"
      "s_waitcnt lgkmcnt(8)\n"
      "s_barrier\n"
      "ds_read_b128 v[34:37], v47\n"
      "s_waitcnt lgkmcnt(7)\n"
      "v_mfma_f32_32x32x16_bf16 v[128:143], a[12:15], v[180:183], v[128:143]\n"
      "ds_read_b64_tr_b16 a[8:9], v26 offset:12288\n"
      "ds_read_b64_tr_b16 a[10:11], v26 offset:12544\n"
      "s_add_u32 m0, s50, 12288\n"
      "s_mov_b32 s66, 86016\n"
      "buffer_load_dwordx4 v6, s[16:19], s66 offen lds\n"
      "s_waitcnt lgkmcnt(7)\n"
      "v_mfma_f32_32x32x16_bf16 v[128:143], a[16:19], v[184:187], v[128:143]\n"
      "ds_read_b64_tr_b16 a[12:13], v27 offset:12288\n"
      "ds_read_b64_tr_b16 a[14:15], v27 offset:12544\n"
      "s_waitcnt lgkmcnt(7)\n"
      "v_mfma_f32_32x32x16_bf16 v[128:143], a[0:3], v[188:191], v[128:143]\n"
      "ds_read_b64_tr_b16 a[16:17], v25 offset:16384\n"
      "ds_read_b64_tr_b16 a[18:19], v25 offset:16640\n"
      "s_add_u32 m0, s50, 13312\n"
      "s_mov_b32 s66, 93184\n"
      "buffer_load_dwordx4 v6, s[16:19], s66 offen lds\n"
      "s_waitcnt lgkmcnt(7)\n"
      "v_mfma_f32_32x32x16_bf16 v[128:143], a[4:7], v[196:199], v[128:143]\n"
      "ds_read_b64_tr_b16 a[0:1], v26 offset:16384\n"
      "ds_read_b64_tr_b16 a[2:3], v26 offset:16640\n"
      "s_waitcnt lgkmcnt(8)\n"
      "v_add_f32 v34, v34, v35\n"
      "v_add_f32 v36, v36, v37\n"
      "v_add_f32 v34, v34, v36\n"
      "s_waitcnt lgkmcnt(6)\n"
      "v_mfma_f32_32x32x16_bf16 v[128:143], a[8:11], v[200:203], v[128:143]\n"
      "ds_read_b64_tr_b16 a[4:5], v27 offset:16384\n"
      "ds_read_b64_tr_b16 a[6:7], v27 offset:16640\n"
      "s_add_u32 m0, s52, 151552\n"
      "s_nop 0\n"
      "buffer_load_dwordx4 v8, s[8:11], 0 offen lds\n"
      "s_waitcnt lgkmcnt(6)\n"
      "v_mfma_f32_32x32x16_bf16 v[128:143], a[12:15], v[204:207], v[128:143]\n"
      "ds_read_b64_tr_b16 a[8:9], v25 offset:20480\n"
      "ds_read_b64_tr_b16 a[10:11], v25 offset:20736\n"
      "v_cndmask_b32 v34, v34, 0, s[74:75]\n"
      "v_cvt_pk_bf16_f32 v53, v34, 0\n"
      "v_lshlrev_b32 v35, 16, v53\n"
      "s_waitcnt lgkmcnt(6)\n"
      "v_mfma_f32_32x32x16_bf16 v[128:143], a[16:19], v[212:215], v[128:143]\n"
      "ds_read_b64_tr_b16 a[12:13], v26 offset:20480\n"
      "ds_read_b64_tr_b16 a[14:15], v26 offset:20736\n"
      "s_add_u32 m0, s52, 152576\n"
      "s_nop 0\n"
      "buffer_load_dwordx4 v14, s[8:11], 0 offen lds\n"
      "s_waitcnt lgkmcnt(6)\n"
      "v_mfma_f32_32x32x16_bf16 v[128:143], a[0:3], v[216:219], v[128:143]\n"
      "ds_read_b64_tr_b16 a[16:17], v27 offset:20480\n"
      "ds_read_b64_tr_b16 a[18:19], v27 offset:20736\n"
      "v_sub_f32 v36, v34, v35\n"
      "v_cvt_pk_bf16_f32 v52, v35, v36\n"
      "s_waitcnt lgkmcnt(6)\n"
      "v_mfma_f32_32x32x16_bf16 v[128:143], a[4:7], v[220:223], v[128:143]\n"
      "ds_read_b64_tr_b16 a[0:1], v25 offset:24576\n"
      "ds_read_b64_tr_b16 a[2:3], v25 offset:24832\n"
      "buffer_load_dwordx4 v[64:67], v5, s[36:39], 0 offen sc1\n"
      "s_waitcnt lgkmcnt(6)\n"
      "v_mfma_f32_32x32x16_bf16 v[128:143], a[8:11], v[228:231], v[128:143]\n"
      "ds_read_b64_tr_b16 a[4:5], v26 offset:24576\n"
      "ds_read_b64_tr_b16 a[6:7], v26 offset:24832\n"
      "s_waitcnt lgkmcnt(6)\n"
      "v_mfma_f32_32x32x16_bf16 v[128:143], a[12:15], v[232:235], v[128:143]\n"
      "ds_read_b64_tr_b16 a[8:9], v27 offset:24576\n"
      "ds_read_b64_tr_b16 a[10:11], v27 offset:24832\n"
      "s_mov_b32 s66, 4096\n"
      "buffer_load_dwordx4 v[68:71], v5, s[36:39], s66 offen sc1\n"
      "s_waitcnt lgkmcnt(6)\n"
      "v_mfma_f32_32x32x16_bf16 v[128:143], a[16:19], v[236:239], v[128:143]\n"
      "s_waitcnt lgkmcnt(4)\n"
      "v_mfma_f32_32x32x16_bf16 v[128:143], a[0:3], v[244:247], v[128:143]\n"
      "s_mov_b32 s66, 8192\n"
      "buffer_load_dwordx4 v[72:75], v5, s[36:39], s66 offen sc1\n"
      "s_waitcnt lgkmcnt(2)\n"
      "v_mfma_f32_32x32x16_bf16 v[128:143], a[4:7], v[248:251], v[128:143]\n"
      "s_waitcnt lgkmcnt(0)\n"
      "v_mfma_f32_32x32x16_bf16 v[128:143], a[8:11], v[252:255], v[128:143]\n"
      "s_mov_b32 s66, 12288\n"
      "buffer_load_dwordx4 v[76:79], v5, s[36:39], s66 offen sc1\n"
      "v_mfma_f32_32x32x16_bf16 v[128:143], v[52:55], v[38:41], v[128:143]\n"
      "s_mov_b32 s66, 16384\n"
      "buffer_load_dwordx4 v[80:83], v5, s[36:39], s66 offen sc1\n"
      "s_mov_b32 s66, 20480\n"
      "buffer_load_dwordx4 v[84:87], v5, s[36:39], s66 offen sc1\n"
      "v_bfe_u32 v2, v1, 2, 2\n"
      "v_cmp_eq_u32 vcc, v2, v10\n"
      "v_and_b32 v2, 1, v1\n"
      "v_lshlrev_b32 v2, 4, v2\n"
      "v_mov_b32 v56, 0x3f80\n"
      "v_lshlrev_b32 v2, v2, v56\n"
      "v_cndmask_b32 v2, 0, v2, vcc\n"
      "v_cmp_gt_u32 vcc, 2, v12\n"
      "v_pk_fma_f32 v[128:129], v[128:129], v[48:49], v[42:43] op_sel:[0,0,0] op_sel_hi:[1,0,0]\n"
      "v_pk_fma_f32 v[130:131], v[130:131], v[48:49], v[42:43] op_sel:[0,0,0] op_sel_hi:[1,0,0]\n"
      "v_pk_fma_f32 v[132:133], v[132:133], v[48:49], v[42:43] op_sel:[0,0,0] op_sel_hi:[1,0,0]\n"
      "v_pk_fma_f32 v[134:135], v[134:135], v[48:49], v[42:43] op_sel:[0,0,0] op_sel_hi:[1,0,0]\n"
      "v_pk_fma_f32 v[136:137], v[136:137], v[48:49], v[42:43] op_sel:[0,0,0] op_sel_hi:[1,0,0]\n"
      "v_pk_fma_f32 v[138:139], v[138:139], v[48:49], v[42:43] op_sel:[0,0,0] op_sel_hi:[1,0,0]\n"
      "v_pk_fma_f32 v[140:141], v[140:141], v[48:49], v[42:43] op_sel:[0,0,0] op_sel_hi:[1,0,0]\n"
      "v_pk_fma_f32 v[142:143], v[142:143], v[48:49], v[42:43] op_sel:[0,0,0] op_sel_hi:[1,0,0]\n"
      "s_mov_b32 s66, 24576\n"
      "buffer_load_dwordx4 v[88:91], v5, s[36:39], s66 offen sc1\n"
      "v_exp_f32_e64 v240, v128 clamp\n"
      "v_max_f32 v128, v128, v42\n"
      "v_exp_f32_e64 v241, v129 clamp\n"
      "v_max_f32 v129, v129, v42\n"
      "v_exp_f32_e64 v242, v130 clamp\n"
      "v_max_f32 v130, v130, v42\n"
      "v_exp_f32_e64 v243, v131 clamp\n"
      "v_max_f32 v131, v131, v42\n"
      "s_mov_b32 s66, 28672\n"
      "buffer_load_dwordx4 v[92:95], v5, s[36:39], s66 offen sc1\n"
      "v_exp_f32_e64 v244, v132 clamp\n"
      "v_max_f32 v132, v132, v42\n"
      "v_exp_f32_e64 v245, v133 clamp\n"
      "v_max_f32 v133, v133, v42\n"
      "v_exp_f32_e64 v246, v134 clamp\n"
      "v_max_f32 v134, v134, v42\n"
      "v_exp_f32_e64 v247, v135 clamp\n"
      "v_max_f32 v135, v135, v42\n"
      "s_mov_b32 s66, 32768\n"
      "buffer_load_dwordx4 v[96:99], v5, s[36:39], s66 offen sc1\n"
      "v_exp_f32_e64 v248, v136 clamp\n"
      "v_max_f32 v136, v136, v42\n"
      "v_exp_f32_e64 v249, v137 clamp\n"
      "v_max_f32 v137, v137, v42\n"
      "v_exp_f32_e64 v250, v138 clamp\n"
      "v_max_f32 v138, v138, v42\n"
      "v_exp_f32_e64 v251, v139 clamp\n"
      "v_max_f32 v139, v139, v42\n"
      "s_mov_b32 s66, 36864\n"
      "buffer_load_dwordx4 v[100:103], v5, s[36:39], s66 offen sc1\n"
      "v_exp_f32_e64 v252, v140 clamp\n"
      "v_max_f32 v140, v140, v42\n"
      "v_exp_f32_e64 v253, v141 clamp\n"
      "v_max_f32 v141, v141, v42\n"
      "v_exp_f32_e64 v254, v142 clamp\n"
      "v_max_f32 v142, v142, v42\n"
      "v_exp_f32_e64 v255, v143 clamp\n"
      "v_max_f32 v143, v143, v42\n"
      "s_mov_b32 s66, 40960\n"
      "buffer_load_dwordx4 v[104:107], v5, s[36:39], s66 offen sc1\n"
      "v_pk_fma_f32 v[128:129], v[128:129], v[42:43], v[240:241] op_sel:[0,1,0] op_sel_hi:[1,1,1]\n"
      "v_pk_fma_f32 v[130:131], v[130:131], v[42:43], v[242:243] op_sel:[0,1,0] op_sel_hi:[1,1,1]\n"
      "v_pk_fma_f32 v[132:133], v[132:133], v[42:43], v[244:245] op_sel:[0,1,0] op_sel_hi:[1,1,1]\n"
      "v_pk_fma_f32 v[134:135], v[134:135], v[42:43], v[246:247] op_sel:[0,1,0] op_sel_hi:[1,1,1]\n"
      "v_pk_fma_f32 v[136:137], v[136:137], v[42:43], v[248:249] op_sel:[0,1,0] op_sel_hi:[1,1,1]\n"
      "v_pk_fma_f32 v[138:139], v[138:139], v[42:43], v[250:251] op_sel:[0,1,0] op_sel_hi:[1,1,1]\n"
      "v_pk_fma_f32 v[140:141], v[140:141], v[42:43], v[252:253] op_sel:[0,1,0] op_sel_hi:[1,1,1]\n"
      "v_pk_fma_f32 v[142:143], v[142:143], v[42:43], v[254:255] op_sel:[0,1,0] op_sel_hi:[1,1,1]\n"
      "v_cvt_pk_bf16_f32 v232, v128, v129\n"
      "v_cvt_pk_bf16_f32 v233, v130, v131\n"
      "v_cvt_pk_bf16_f32 v234, v132, v133\n"
      "v_cvt_pk_bf16_f32 v235, v134, v135\n"
      "v_cvt_pk_bf16_f32 v236, v136, v137\n"
      "v_cvt_pk_bf16_f32 v237, v138, v139\n"
      "v_cvt_pk_bf16_f32 v238, v140, v141\n"
      "v_cvt_pk_bf16_f32 v239, v142, v143\n"
      "s_mov_b32 s66, 45056\n"
      "buffer_load_dwordx4 v[108:111], v5, s[36:39], s66 offen sc1\n"
      "v_cndmask_b32 v238, v238, 0, s[74:75]\n"
      "v_cndmask_b32 v239, v239, 0, s[74:75]\n"
      "s_mov_b32 s66, 49152\n"
      "buffer_load_dwordx4 v[112:115], v5, s[36:39], s66 offen sc1\n"
      "v_mov_b32 v56, 0\n"
      "v_mov_b32 v58, 0\n"
      "v_cndmask_b32 v57, 0, v2, vcc\n"
      "v_sub_u32 v59, v2, v57\n"
      "s_mov_b32 s66, 53248\n"
      "buffer_load_dwordx4 v[116:119], v5, s[36:39], s66 offen sc1\n"
      "s_lshr_b32 s46, s33, 1\n"
      "s_lshl_b32 s46, s46, 10\n"
      "s_and_b32 s47, s33, 1\n"
      "s_lshl3_add_u32 s46, s47, s46\n"
      "s_mov_b32 s66, 57344\n"
      "buffer_load_dwordx4 v[120:123], v5, s[36:39], s66 offen sc1\n"
      "v_add_u32 v4, 161792, v28\n"
      "v_add_u32 v3, s46, v4\n"
      "s_mov_b32 s66, 61440\n"
      "buffer_load_dwordx4 v[124:127], v5, s[36:39], s66 offen sc1\n"
      "s_mov_b32 s10, 50000\n"
      ".Lk1_kvchk_%=:\n"
      "v_mov_b32 v11, 0\n"
      "s_waitcnt vmcnt(15)\n"
      "v_mfma_f32_16x16x32_bf16 v[24:27], v[56:59], v[64:67], 0\n"
      "v_bitop3_b32 v2, v64, v66, s73 bitop3:0x7e\n"
      "v_or_b32 v11, v11, v2\n"
      "s_waitcnt vmcnt(14)\n"
      "v_mfma_f32_16x16x32_bf16 v[24:27], v[56:59], v[68:71], v[24:27]\n"
      "v_bitop3_b32 v2, v68, v70, s73 bitop3:0x7e\n"
      "v_or_b32 v11, v11, v2\n"
      "s_waitcnt vmcnt(13)\n"
      "v_mfma_f32_16x16x32_bf16 v[24:27], v[56:59], v[72:75], v[24:27]\n"
      "v_bitop3_b32 v2, v72, v74, s73 bitop3:0x7e\n"
      "v_or_b32 v11, v11, v2\n"
      "s_waitcnt vmcnt(12)\n"
      "v_mfma_f32_16x16x32_bf16 v[24:27], v[56:59], v[76:79], v[24:27]\n"
      "v_bitop3_b32 v2, v76, v78, s73 bitop3:0x7e\n"
      "v_or_b32 v11, v11, v2\n"
      "s_waitcnt vmcnt(11)\n"
      "v_mfma_f32_16x16x32_bf16 v[24:27], v[56:59], v[80:83], v[24:27]\n"
      "v_bitop3_b32 v2, v80, v82, s73 bitop3:0x7e\n"
      "v_or_b32 v11, v11, v2\n"
      "s_waitcnt vmcnt(10)\n"
      "v_mfma_f32_16x16x32_bf16 v[24:27], v[56:59], v[84:87], v[24:27]\n"
      "v_bitop3_b32 v2, v84, v86, s73 bitop3:0x7e\n"
      "v_or_b32 v11, v11, v2\n"
      "s_waitcnt vmcnt(9)\n"
      "v_mfma_f32_16x16x32_bf16 v[24:27], v[56:59], v[88:91], v[24:27]\n"
      "v_bitop3_b32 v2, v88, v90, s73 bitop3:0x7e\n"
      "v_or_b32 v11, v11, v2\n"
      "s_waitcnt vmcnt(8)\n"
      "v_mfma_f32_16x16x32_bf16 v[24:27], v[56:59], v[92:95], v[24:27]\n"
      "v_bitop3_b32 v2, v92, v94, s73 bitop3:0x7e\n"
      "v_or_b32 v11, v11, v2\n"
      "s_waitcnt vmcnt(7)\n"
      "v_mfma_f32_16x16x32_bf16 v[24:27], v[56:59], v[96:99], v[24:27]\n"
      "v_bitop3_b32 v2, v96, v98, s73 bitop3:0x7e\n"
      "v_or_b32 v11, v11, v2\n"
      "s_waitcnt vmcnt(6)\n"
      "v_mfma_f32_16x16x32_bf16 v[24:27], v[56:59], v[100:103], v[24:27]\n"
      "v_bitop3_b32 v2, v100, v102, s73 bitop3:0x7e\n"
      "v_or_b32 v11, v11, v2\n"
      "s_waitcnt vmcnt(5)\n"
      "v_mfma_f32_16x16x32_bf16 v[24:27], v[56:59], v[104:107], v[24:27]\n"
      "v_bitop3_b32 v2, v104, v106, s73 bitop3:0x7e\n"
      "v_or_b32 v11, v11, v2\n"
      "s_waitcnt vmcnt(4)\n"
      "v_mfma_f32_16x16x32_bf16 v[24:27], v[56:59], v[108:111], v[24:27]\n"
      "v_bitop3_b32 v2, v108, v110, s73 bitop3:0x7e\n"
      "v_or_b32 v11, v11, v2\n"
      "s_waitcnt vmcnt(3)\n"
      "v_mfma_f32_16x16x32_bf16 v[24:27], v[56:59], v[112:115], v[24:27]\n"
      "v_bitop3_b32 v2, v112, v114, s73 bitop3:0x7e\n"
      "v_or_b32 v11, v11, v2\n"
      "s_waitcnt vmcnt(2)\n"
      "v_mfma_f32_16x16x32_bf16 v[24:27], v[56:59], v[116:119], v[24:27]\n"
      "v_bitop3_b32 v2, v116, v118, s73 bitop3:0x7e\n"
      "v_or_b32 v11, v11, v2\n"
      "s_waitcnt vmcnt(1)\n"
      "v_mfma_f32_16x16x32_bf16 v[24:27], v[56:59], v[120:123], v[24:27]\n"
      "v_bitop3_b32 v2, v120, v122, s73 bitop3:0x7e\n"
      "v_or_b32 v11, v11, v2\n"
      "s_waitcnt vmcnt(0)\n"
      "v_mfma_f32_16x16x32_bf16 v[24:27], v[56:59], v[124:127], v[24:27]\n"
      "v_bitop3_b32 v2, v124, v126, s73 bitop3:0x7e\n"
      "v_or_b32 v11, v11, v2\n"
      "v_cmp_ne_u32 vcc, 0, v11\n"
      "s_nop 0\n"
      "s_cbranch_vccnz .Lk3_kvslow_%=\n"
      ".Lk2_kvok_%=:\n"
      "s_nop 2\n"
      "v_cvt_pk_bf16_f32 v18, v24, v25\n"
      "v_cvt_pk_bf16_f32 v19, v26, v27\n"
      "ds_write_b64 v3, v[18:19]\n"
      "s_mul_i32 s50, s33, 14336\n"
      "v_mul_u32_u24 v5, 3584, v22\n"
      "v_lshl_add_u32 v2, v10, 2, v12\n"
      "v_lshlrev_b32 v2, 4, v2\n"
      "s_lshl_b32 s46, s69, 8\n"
      "s_lshl1_add_u32 s46, s50, s46\n"
      "v_add3_u32 v5, v5, v2, s46\n"
      "s_waitcnt lgkmcnt(0)\n"
      "s_barrier\n"
      "ds_read_b128 v[224:227], v4\n"
      "ds_read_b128 v[228:231], v4 offset:1024\n"
      "s_xor_b32 s48, s72, 0\n"
      "s_mul_i32 s11, s48, 229376\n"
      "s_lshl_b32 s12, s48, 14\n"
      "s_add_u32 s12, s12, s52\n"
      "s_add_u32 m0, s12, 57344\n"
      "s_nop 0\n"
      "buffer_load_dwordx4 v5, s[20:23], s11 offen lds\n"
      "v_and_b32 v2, 1, v10\n"
      "v_lshlrev_b32 v2, 14, v2\n"
      "v_lshl_add_u32 v2, v17, 8, v2\n"
      "v_and_b32 v6, 0xf0, v28\n"
      "s_lshl_b32 s46, s65, 15\n"
      "s_lshl_b32 s47, s45, 10\n"
      "s_add_u32 s46, s46, s47\n"
      "s_add_u32 s46, s46, 1572864\n"
      "v_add3_u32 v6, v6, v2, s46\n"
      "s_waitcnt lgkmcnt(1)\n"
      "v_mfma_f32_32x32x16_bf16 v[240:255], v[224:227], v[232:235], 0\n"
      "s_waitcnt lgkmcnt(0)\n"
      "v_mfma_f32_32x32x16_bf16 v[240:255], v[228:231], v[236:239], v[240:255]\n"
      "s_add_u32 m0, s12, 58368\n"
      "s_add_u32 s66, s11, 14336\n"
      "buffer_load_dwordx4 v5, s[20:23], s66 offen lds\n"
      "v_mov_b32 v8, s73\n"
      "s_lshl_b32 s46, s33, 2\n"
      "s_lshl_b32 s46, 15, s46\n"
      "s_and_b32 s8, s46, 0x3fff\n"
      "s_mov_b32 s9, 0\n"
      "s_mul_i32 s46, s44, 1792\n"
      "s_lshl2_add_u32 s46, s45, s46\n"
      "s_add_u32 s46, s46, 4096\n"
      "v_lshl_add_u32 v7, v1, 7, s46\n"
      "v_mov_b32 v14, v252\n"
      "v_mov_b32 v15, v252\n"
      "s_nop 1\n"
      "v_permlane32_swap_b32 v15, v14\n"
      "v_add_f32 v14, 0x358637bd, v14\n"
      "v_rcp_f32 v14, v14\n"
      "s_nop 0\n"
      "v_mul_f32 v14, 0x3f317218, v14\n"
      "v_pk_mul_f32 v[240:241], v[240:241], v[14:15] op_sel:[0,0] op_sel_hi:[1,0]\n"
      "v_cvt_pk_bf16_f32 v232, v240, v241\n"
      "v_pk_mul_f32 v[242:243], v[242:243], v[14:15] op_sel:[0,0] op_sel_hi:[1,0]\n"
      "v_cvt_pk_bf16_f32 v233, v242, v243\n"
      "v_pk_mul_f32 v[244:245], v[244:245], v[14:15] op_sel:[0,0] op_sel_hi:[1,0]\n"
      "v_cvt_pk_bf16_f32 v234, v244, v245\n"
      "v_pk_mul_f32 v[246:247], v[246:247], v[14:15] op_sel:[0,0] op_sel_hi:[1,0]\n"
      "v_cvt_pk_bf16_f32 v235, v246, v247\n"
      "buffer_store_dwordx4 v[232:235], v6, s[36:39], 0 offen sc1\n"
      "v_pk_mul_f32 v[248:249], v[248:249], v[14:15] op_sel:[0,0] op_sel_hi:[1,0]\n"
      "v_cvt_pk_bf16_f32 v236, v248, v249\n"
      "v_pk_mul_f32 v[250:251], v[250:251], v[14:15] op_sel:[0,0] op_sel_hi:[1,0]\n"
      "v_cvt_pk_bf16_f32 v237, v250, v251\n"
      "v_pk_mul_f32 v[252:253], v[252:253], v[14:15] op_sel:[0,0] op_sel_hi:[1,0]\n"
      "v_cvt_pk_bf16_f32 v238, v252, v253\n"
      "v_pk_mul_f32 v[254:255], v[254:255], v[14:15] op_sel:[0,0] op_sel_hi:[1,0]\n"
      "v_cvt_pk_bf16_f32 v239, v254, v255\n"
      "v_cndmask_b32 v238, v238, 0, s[74:75]\n"
      "v_cndmask_b32 v239, v239, 0, s[74:75]\n"
      "buffer_store_dwordx4 v[236:239], v6, s[36:39], 0 offen offset:512 sc1\n"
      "s_cmp_ge_u32 s43, 28\n"
      "s_cbranch_scc1 .Lk4_helper_%=\n"
      "s_add_u32 m0, s12, 65536\n"
      "s_add_u32 s66, s11, 114688\n"
      "buffer_load_dwordx4 v5, s[20:23], s66 offen lds\n"
      "s_mul_i32 s46, s70, 7\n"
      "s_add_u32 s46, s46, s71\n"
      "s_lshl_b32 s46, s46, 7\n"
      "s_add_u32 s46, s46, 4096\n"
      "v_lshl_add_u32 v4, v1, 2, s46\n"
      "v_lshrrev_b32 v24, 1, v10\n"
      "v_and_b32 v25, 1, v10\n"
      "v_lshl_add_u32 v24, v24, 2, v25\n"
      "v_lshl_add_u32 v24, v24, 2, v22\n"
      "v_lshrrev_b32 v2, 1, v22\n"
      "v_lshl_add_u32 v25, v25, 1, v2\n"
      "v_lshl_add_u32 v25, v25, 2, v12\n"
      "s_add_u32 m0, s12, 66560\n"
      "s_add_u32 s66, s11, 129024\n"
      "buffer_load_dwordx4 v5, s[20:23], s66 offen lds\n"
      "v_lshlrev_b32 v25, 3, v25\n"
      "v_lshl_add_u32 v6, v24, 7, v25\n"
      "v_xor_b32 v11, 32, v6\n"
      "v_xor_b32 v13, 64, v6\n"
      "v_xor_b32 v14, 96, v6\n"
      "v_min_u32 v21, 2, v10\n"
      "v_lshrrev_b32 v24, 1, v21\n"
      "v_and_b32 v25, 1, v21\n"
      "v_lshl_add_u32 v24, v24, 2, v25\n"
      "v_lshl_add_u32 v24, v24, 2, v22\n"
      "v_lshrrev_b32 v2, 1, v22\n"
      "v_lshl_add_u32 v25, v25, 1, v2\n"
      "s_xor_b32 s48, s72, 1\n"
      "s_mul_i32 s11, s48, 229376\n"
      "s_lshl_b32 s12, s48, 14\n"
      "s_add_u32 s12, s12, s52\n"
      "s_add_u32 m0, s12, 57344\n"
      "s_nop 0\n"
      "buffer_load_dwordx4 v5, s[20:23], s11 offen lds\n"
      "v_lshl_add_u32 v25, v25, 2, v12\n"
      "v_lshlrev_b32 v25, 3, v25\n"
      "v_lshl_add_u32 v15, v24, 7, v25\n"
      "v_xor_b32 v18, 32, v15\n"
      "v_xor_b32 v19, 64, v15\n"
      "v_xor_b32 v20, 96, v15\n"
      "s_add_u32 m0, s12, 58368\n"
      "s_add_u32 s66, s11, 14336\n"
      "buffer_load_dwordx4 v5, s[20:23], s66 offen lds\n"
      "ds_read_b64_tr_b16 v[144:145], v6 offset:0\n"
      "s_lshl2_add_u32 s46, s69, s33\n"
      "ds_read_b64_tr_b16 v[146:147], v15 offset:1024\n"
      "s_lshl_b32 s46, s46, 6\n"
      "ds_read_b64_tr_b16 v[148:149], v11 offset:0\n"
      "v_lshl_add_u32 v2, v16, 1, s46\n"
      "ds_read_b64_tr_b16 v[150:151], v18 offset:1024\n"
      "global_load_ushort v27, v2, s[34:35]\n"
      "ds_read_b64_tr_b16 v[152:153], v13 offset:0\n"
      "s_lshl2_add_u32 s46, s71, s33\n"
      "ds_read_b64_tr_b16 v[154:155], v19 offset:1024\n"
      "ds_read_b64_tr_b16 v[156:157], v14 offset:0\n"
      "s_add_u32 m0, s12, 65536\n"
      "s_add_u32 s66, s11, 114688\n"
      "buffer_load_dwordx4 v5, s[20:23], s66 offen lds\n"
      "ds_read_b64_tr_b16 v[158:159], v20 offset:1024\n"
      "s_lshl_b32 s46, s46, 5\n"
      "ds_read_b64_tr_b16 v[160:161], v13 offset:3584\n"
      "v_lshl_add_u32 v2, v17, 3, s46\n"
      "ds_read_b64_tr_b16 v[162:163], v19 offset:4608\n"
      "global_load_dwordx2 v[34:35], v2, s[40:41]\n"
      "ds_read_b64_tr_b16 v[164:165], v14 offset:3584\n"
      "global_load_dwordx2 v[36:37], v2, s[40:41] offset:16\n"
      "ds_read_b64_tr_b16 v[166:167], v20 offset:4608\n"
      "v_and_b32 v26, 15, v1\n"
      "ds_read_b64_tr_b16 v[168:169], v6 offset:3584\n"
      "ds_read_b64_tr_b16 v[170:171], v15 offset:4608\n"
      "s_add_u32 m0, s12, 66560\n"
      "s_add_u32 s66, s11, 129024\n"
      "buffer_load_dwordx4 v5, s[20:23], s66 offen lds\n"
      "ds_read_b64_tr_b16 v[172:173], v11 offset:3584\n"
      "v_bfe_u32 v2, v1, 1, 3\n"
      "ds_read_b64_tr_b16 v[174:175], v18 offset:4608\n"
      "v_xor_b32 v2, v2, v17\n"
      "ds_read_b64_tr_b16 v[176:177], v6 offset:7168\n"
      "v_lshlrev_b32 v2, 4, v2\n"
      "ds_read_b64_tr_b16 v[178:179], v15 offset:8192\n"
      "v_lshl_add_u32 v2, v26, 7, v2\n"
      "ds_read_b64_tr_b16 v[180:181], v11 offset:7168\n"
      "v_and_b32 v21, 1, v10\n"
      "ds_read_b64_tr_b16 v[182:183], v18 offset:8192\n"
      "ds_read_b64_tr_b16 v[184:185], v13 offset:7168\n"
      "s_xor_b32 s48, s72, 2\n"
      "s_mul_i32 s11, s48, 229376\n"
      "s_lshl_b32 s12, s48, 14\n"
      "s_add_u32 s12, s12, s52\n"
      "s_add_u32 m0, s12, 57344\n"
      "s_nop 0\n"
      "buffer_load_dwordx4 v5, s[20:23], s11 offen lds\n"
      "ds_read_b64_tr_b16 v[186:187], v19 offset:8192\n"
      "v_lshl_add_u32 v2, v21, 3, v2\n"
      "ds_read_b64_tr_b16 v[188:189], v14 offset:7168\n"
      "s_add_u32 s46, s52, 151552\n"
      "ds_read_b64_tr_b16 v[190:191], v20 offset:8192\n"
      "v_add_u32 v21, s46, v2\n"
      "ds_read_b64_tr_b16 v[192:193], v13 offset:10752\n"
      "v_xor_b32 v2, 0, v21\n"
      "ds_read_b64_tr_b16 v[194:195], v19 offset:11776\n"
      "ds_read_b64 v[120:121], v2\n"
      "ds_read_b64_tr_b16 v[196:197], v14 offset:10752\n"
      "ds_read_b64_tr_b16 v[198:199], v20 offset:11776\n"
      "s_add_u32 m0, s12, 58368\n"
      "s_add_u32 s66, s11, 14336\n"
      "buffer_load_dwordx4 v5, s[20:23], s66 offen lds\n"
      "ds_read_b64_tr_b16 v[200:201], v6 offset:10752\n"
      "v_xor_b32 v2, 32, v21\n"
      "ds_read_b64_tr_b16 v[202:203], v15 offset:11776\n"
      "ds_read_b64 v[122:123], v2\n"
      "ds_read_b64_tr_b16 v[204:205], v11 offset:10752\n"
      "v_xor_b32 v2, 64, v21\n"
      "ds_read_b64_tr_b16 v[206:207], v18 offset:11776\n"
      "ds_read_b64 v[124:125], v2\n"
      "ds_read_b64_tr_b16 v[208:209], v6 offset:14336\n"
      "v_xor_b32 v2, 96, v21\n"
      "ds_read_b64_tr_b16 v[210:211], v15 offset:15360\n"
      "ds_read_b64_tr_b16 v[212:213], v11 offset:14336\n"
      "s_add_u32 m0, s12, 65536\n"
      "s_add_u32 s66, s11, 114688\n"
      "buffer_load_dwordx4 v5, s[20:23], s66 offen lds\n"
      "ds_read_b64_tr_b16 v[214:215], v18 offset:15360\n"
      "ds_read_b64 v[126:127], v2\n"
      "ds_read_b64_tr_b16 v[216:217], v13 offset:14336\n"
      "s_lshl_b32 s46, s51, 14\n"
      "ds_read_b64_tr_b16 v[218:219], v19 offset:15360\n"
      "s_add_u32 s46, s46, 1572864\n"
      "ds_read_b64_tr_b16 v[220:221], v14 offset:14336\n"
      "v_add_u32 v29, s46, v28\n"
      "ds_read_b64_tr_b16 v[222:223], v20 offset:15360\n"
      "s_lshr_b32 s13, s51, 1\n"
      "ds_read_b64_tr_b16 v[224:225], v13 offset:17920\n"
      "ds_read_b64_tr_b16 v[226:227], v19 offset:18944\n"
      "s_waitcnt vmcnt(12)\n"
      "s_barrier\n"
      "s_mov_b64 exec, s[8:9]\n"
      "buffer_atomic_swap v8, v7, s[36:39], 0 offen offset:0\n"
      "s_mov_b64 exec, -1\n"
      "s_add_u32 m0, s12, 66560\n"
      "s_add_u32 s66, s11, 129024\n"
      "buffer_load_dwordx4 v5, s[20:23], s66 offen lds\n"
      "ds_read_b64_tr_b16 v[228:229], v14 offset:17920\n"
      "s_mul_i32 s46, s13, 28672\n"
      "ds_read_b64_tr_b16 v[230:231], v20 offset:18944\n"
      "s_lshl_b32 s47, s71, 12\n"
      "ds_read_b64_tr_b16 v[232:233], v6 offset:17920\n"
      "s_add_u32 s46, s46, s47\n"
      "ds_read_b64_tr_b16 v[234:235], v15 offset:18944\n"
      "s_and_b32 s51, s33, 1\n"
      "ds_read_b64_tr_b16 v[236:237], v11 offset:17920\n"
      "s_lshl_b32 s47, s51, 8\n"
      "ds_read_b64_tr_b16 v[238:239], v18 offset:18944\n"
      "ds_read_b64_tr_b16 v[240:241], v6 offset:21504\n"
      "s_xor_b32 s48, s72, 3\n"
      "s_mul_i32 s11, s48, 229376\n"
      "s_lshl_b32 s12, s48, 14\n"
      "s_add_u32 s12, s12, s52\n"
      "s_add_u32 m0, s12, 57344\n"
      "s_nop 0\n"
      "buffer_load_dwordx4 v5, s[20:23], s11 offen lds\n"
      "ds_read_b64_tr_b16 v[242:243], v15 offset:22528\n"
      "s_add_u32 s46, s46, s47\n"
      "ds_read_b64_tr_b16 v[244:245], v11 offset:21504\n"
      "s_add_u32 s46, s46, 3670016\n"
      "ds_read_b64_tr_b16 v[246:247], v18 offset:22528\n"
      "v_lshlrev_b32 v2, 9, v10\n"
      "ds_read_b64_tr_b16 v[248:249], v13 offset:21504\n"
      "v_lshl_add_u32 v2, v26, 4, v2\n"
      "ds_read_b64_tr_b16 v[250:251], v19 offset:22528\n"
      "v_add_u32 v38, s46, v2\n"
      "ds_read_b64_tr_b16 v[252:253], v14 offset:21504\n"
      "ds_read_b64_tr_b16 v[254:255], v20 offset:22528\n"
      "v_mov_b32 v3, v8\n"
      "s_mov_b64 exec, 0xffff\n"
      "v_mov_b32 v3, 0\n"
      "buffer_atomic_or v3, v4, s[36:39], 0 offen offset:0 sc0\n"
      "s_mov_b64 exec, -1\n"
      "s_add_u32 m0, s12, 58368\n"
      "s_add_u32 s66, s11, 14336\n"
      "buffer_load_dwordx4 v5, s[20:23], s66 offen lds\n"
      "ds_read_b64_tr_b16 a[112:113], v13 offset:25088\n"
      "s_lshl_b32 s46, s13, 11\n"
      "ds_read_b64_tr_b16 a[114:115], v19 offset:26112\n"
      "s_lshl_b32 s47, s71, 8\n"
      "ds_read_b64_tr_b16 a[116:117], v14 offset:25088\n"
      "s_add_u32 s46, s46, s47\n"
      "ds_read_b64_tr_b16 a[118:119], v20 offset:26112\n"
      "s_lshl_b32 s47, s51, 6\n"
      "ds_read_b64_tr_b16 a[120:121], v6 offset:25088\n"
      "s_add_u32 s46, s46, s47\n"
      "ds_read_b64_tr_b16 a[122:123], v15 offset:26112\n"
      "ds_read_b64_tr_b16 a[124:125], v11 offset:25088\n"
      "s_add_u32 m0, s12, 65536\n"
      "s_add_u32 s66, s11, 114688\n"
      "buffer_load_dwordx4 v5, s[20:23], s66 offen lds\n"
      "ds_read_b64_tr_b16 a[126:127], v18 offset:26112\n"
      "s_add_u32 s46, s46, 348160\n"
      "ds_read_b64_tr_b16 a[128:129], v6 offset:28672\n"
      "v_lshlrev_b32 v2, 7, v17\n"
      "ds_read_b64_tr_b16 a[130:131], v15 offset:29696\n"
      "v_lshl_add_u32 v2, v26, 2, v2\n"
      "ds_read_b64_tr_b16 a[132:133], v11 offset:28672\n"
      "v_add_u32 v39, s46, v2\n"
      "ds_read_b64_tr_b16 a[134:135], v18 offset:29696\n"
      "s_mul_i32 s46, s68, 1792\n"
      "ds_read_b64_tr_b16 a[136:137], v13 offset:28672\n"
      "ds_read_b64_tr_b16 a[138:139], v19 offset:29696\n"
      "s_add_u32 m0, s12, 66560\n"
      "s_add_u32 s66, s11, 129024\n"
      "buffer_load_dwordx4 v5, s[20:23], s66 offen lds\n"
      "ds_read_b64_tr_b16 a[140:141], v14 offset:28672\n"
      "s_lshl2_add_u32 s46, s69, s46\n"
      "ds_read_b64_tr_b16 a[142:143], v20 offset:29696\n"
      "s_add_u32 s46, s46, 118784\n"
      "ds_read_b64_tr_b16 a[144:145], v13 offset:32256\n"
      "v_lshl_add_u32 v40, v1, 7, s46\n"
      "ds_read_b64_tr_b16 a[146:147], v19 offset:33280\n"
      "v_lshl_add_u32 v2, v17, 4, v22\n"
      "ds_read_b64_tr_b16 a[148:149], v14 offset:32256\n"
      "v_lshlrev_b32 v2, 6, v2\n"
      "ds_read_b64_tr_b16 a[150:151], v20 offset:33280\n"
      "ds_read_b64_tr_b16 a[152:153], v6 offset:32256\n"
      "s_add_u32 m0, s52, 122880\n"
      "s_mov_b32 s66, 917504\n"
      "buffer_load_dwordx4 v5, s[20:23], s66 offen lds\n"
      "ds_read_b64_tr_b16 a[154:155], v15 offset:33280\n"
      "v_lshl_add_u32 v2, v23, 3, v2\n"
      "ds_read_b64_tr_b16 a[156:157], v11 offset:32256\n"
      "s_lshl_b32 s46, s33, 8\n"
      "ds_read_b64_tr_b16 a[158:159], v18 offset:33280\n"
      "s_add_u32 s46, s46, 57344\n"
      "ds_read_b64_tr_b16 a[160:161], v6 offset:35840\n"
      "v_add_u32 v41, s46, v2\n"
      "ds_read_b64_tr_b16 a[162:163], v15 offset:36864\n"
      "v_add_u32 v42, 49152, v41\n"
      "ds_read_b64_tr_b16 a[164:165], v11 offset:35840\n"
      "ds_read_b64_tr_b16 a[166:167], v18 offset:36864\n"
      "s_add_u32 m0, s52, 123904\n"
      "s_mov_b32 s66, 931840\n"
      "buffer_load_dwordx4 v5, s[20:23], s66 offen lds\n"
      "ds_read_b64_tr_b16 a[168:169], v13 offset:35840\n"
      "v_lshl_add_u32 v2, v17, 2, v22\n"
      "ds_read_b64_tr_b16 a[170:171], v19 offset:36864\n"
      "v_mul_u32_u24 v43, 896, v2\n"
      "ds_read_b64_tr_b16 a[172:173], v14 offset:35840\n"
      "s_lshl_b32 s47, s33, 1\n"
      "ds_read_b64_tr_b16 a[174:175], v20 offset:36864\n"
      "v_xor_b32 v2, s47, v23\n"
      "ds_read_b64_tr_b16 a[176:177], v13 offset:39424\n"
      "v_lshlrev_b32 v2, 4, v2\n"
      "ds_read_b64_tr_b16 a[178:179], v19 offset:40448\n"
      "ds_read_b64_tr_b16 a[180:181], v14 offset:39424\n"
      "s_add_u32 m0, s52, 131072\n"
      "s_mov_b32 s66, 1032192\n"
      "buffer_load_dwordx4 v5, s[20:23], s66 offen lds\n"
      "ds_read_b64_tr_b16 a[182:183], v20 offset:40448\n"
      "s_lshl_b32 s46, s71, 7\n"
      "ds_read_b64_tr_b16 a[184:185], v6 offset:39424\n"
      "s_lshl1_add_u32 s46, s50, s46\n"
      "ds_read_b64_tr_b16 a[186:187], v15 offset:40448\n"
      "v_add3_u32 v43, v43, v2, s46\n"
      "ds_read_b64_tr_b16 a[188:189], v11 offset:39424\n"
      "v_lshl_add_u32 v2, v17, 3, v22\n"
      "ds_read_b64_tr_b16 a[190:191], v18 offset:40448\n"
      "v_lshlrev_b32 v2, 6, v2\n"
      "ds_read_b64_tr_b16 a[192:193], v6 offset:43008\n"
      "ds_read_b64_tr_b16 a[194:195], v15 offset:44032\n"
      "s_add_u32 m0, s52, 132096\n"
      "s_mov_b32 s66, 1046528\n"
      "buffer_load_dwordx4 v5, s[20:23], s66 offen lds\n"
      "ds_read_b64_tr_b16 a[196:197], v11 offset:43008\n"
      "v_lshl_add_u32 v44, v23, 3, v2\n"
      "ds_read_b64_tr_b16 a[198:199], v18 offset:44032\n"
      "s_waitcnt lgkmcnt(14)\n"
      "v_lshlrev_b32 v128, 16, v120\n"
      "ds_read_b64_tr_b16 a[200:201], v13 offset:43008\n"
      "v_and_b32 v129, 0xffff0000, v120\n"
      "ds_read_b64_tr_b16 a[202:203], v19 offset:44032\n"
      "v_lshlrev_b32 v130, 16, v121\n"
      "ds_read_b64_tr_b16 a[204:205], v14 offset:43008\n"
      "v_and_b32 v131, 0xffff0000, v121\n"
      "ds_read_b64_tr_b16 a[206:207], v20 offset:44032\n"
      "ds_read_b64_tr_b16 a[208:209], v13 offset:46592\n"
      "s_add_u32 m0, s52, 139264\n"
      "s_mov_b32 s66, 1146880\n"
      "buffer_load_dwordx4 v5, s[20:23], s66 offen lds\n"
      "ds_read_b64_tr_b16 a[210:211], v19 offset:47616\n"
      "v_lshlrev_b32 v132, 16, v122\n"
      "ds_read_b64_tr_b16 a[212:213], v14 offset:46592\n"
      "v_and_b32 v133, 0xffff0000, v122\n"
      "ds_read_b64_tr_b16 a[214:215], v20 offset:47616\n"
      "v_lshlrev_b32 v134, 16, v123\n"
      "ds_read_b64_tr_b16 a[216:217], v6 offset:46592\n"
      "v_and_b32 v135, 0xffff0000, v123\n"
      "ds_read_b64_tr_b16 a[218:219], v15 offset:47616\n"
      "v_lshlrev_b32 v136, 16, v124\n"
      "ds_read_b64_tr_b16 a[220:221], v11 offset:46592\n"
      "ds_read_b64_tr_b16 a[222:223], v18 offset:47616\n"
      "s_add_u32 m0, s52, 140288\n"
      "s_mov_b32 s66, 1161216\n"
      "buffer_load_dwordx4 v5, s[20:23], s66 offen lds\n"
      "ds_read_b64_tr_b16 a[224:225], v6 offset:50176\n"
      "v_and_b32 v137, 0xffff0000, v124\n"
      "ds_read_b64_tr_b16 a[226:227], v15 offset:51200\n"
      "v_lshlrev_b32 v138, 16, v125\n"
      "ds_read_b64_tr_b16 a[228:229], v11 offset:50176\n"
      "v_and_b32 v139, 0xffff0000, v125\n"
      "ds_read_b64_tr_b16 a[230:231], v18 offset:51200\n"
      "v_lshlrev_b32 v140, 16, v126\n"
      "ds_read_b64_tr_b16 a[232:233], v13 offset:50176\n"
      "v_and_b32 v141, 0xffff0000, v126\n"
      "ds_read_b64_tr_b16 a[234:235], v19 offset:51200\n"
      "ds_read_b64_tr_b16 a[236:237], v14 offset:50176\n"
      "ds_read_b64_tr_b16 a[238:239], v20 offset:51200\n"
      "v_lshlrev_b32 v142, 16, v127\n"
      "ds_read_b64_tr_b16 a[240:241], v13 offset:53760\n"
      "v_and_b32 v143, 0xffff0000, v127\n"
      "ds_read_b64_tr_b16 a[242:243], v19 offset:54784\n"
      "ds_read_b64_tr_b16 a[244:245], v14 offset:53760\n"
      "ds_read_b64_tr_b16 a[246:247], v20 offset:54784\n"
      "ds_read_b64_tr_b16 a[248:249], v6 offset:53760\n"
      "ds_read_b64_tr_b16 a[250:251], v15 offset:54784\n"
      "ds_read_b64_tr_b16 a[252:253], v11 offset:53760\n"
      "ds_read_b64_tr_b16 a[254:255], v18 offset:54784\n"
      "s_waitcnt vmcnt(9)\n"
      "v_cmp_ne_u32 vcc, s73, v3\n"
      "s_nop 0\n"
      "s_cbranch_vccnz .Lk6_p2slow_%=\n"
      ".Lk7_p2back_%=:\n"
      "buffer_load_dwordx4 v[64:67], v29, s[36:39], 0 offen offset:0 sc1\n"
      "buffer_load_dwordx4 v[68:71], v29, s[36:39], 0 offen offset:1024 sc1\n"
      "buffer_load_dwordx4 v[72:75], v29, s[36:39], 0 offen offset:2048 sc1\n"
      "buffer_load_dwordx4 v[76:79], v29, s[36:39], 0 offen offset:3072 sc1\n"
      "s_mov_b32 s13, 4096\n"
      "buffer_load_dwordx4 v[80:83], v29, s[36:39], s13 offen offset:0 sc1\n"
      "buffer_load_dwordx4 v[84:87], v29, s[36:39], s13 offen offset:1024 sc1\n"
      "buffer_load_dwordx4 v[88:91], v29, s[36:39], s13 offen offset:2048 sc1\n"
      "s_or_b32 s46, %1, s33\n"
      "s_cmp_lg_u32 s46, 0\n"
      "s_cselect_b32 s46, -1, 0\n"
      "v_mov_b32 v3, s46\n"
      "v_mov_b32 v7, s49\n"
      "s_waitcnt lgkmcnt(0)\n"
      "s_waitcnt vmcnt(7)\n"
      "s_barrier\n"
      "ds_read_b64_tr_b16 a[0:1], v41 offset:0\n"
      "ds_read_b64_tr_b16 a[2:3], v41 offset:4096\n"
      "ds_read_b64_tr_b16 a[4:5], v41 offset:2048\n"
      "ds_read_b64_tr_b16 a[6:7], v41 offset:6144\n"
      "ds_read_b64_tr_b16 a[8:9], v41 offset:8192\n"
      "ds_read_b64_tr_b16 a[10:11], v41 offset:12288\n"
      "ds_read_b64_tr_b16 a[12:13], v41 offset:10240\n"
      "ds_read_b64_tr_b16 a[14:15], v41 offset:14336\n"
      "s_waitcnt vmcnt(6)\n"
      "v_mfma_f32_16x16x32_bf16 v[128:131], v[144:147], v[64:67], v[128:131]\n"
      "buffer_load_dwordx4 v[92:95], v29, s[36:39], s13 offen offset:3072 sc1\n"
      "v_mfma_f32_16x16x32_bf16 v[132:135], v[148:151], v[64:67], v[132:135]\n"
      "ds_read_b64_tr_b16 a[16:17], v41 offset:16384\n"
      "v_mfma_f32_16x16x32_bf16 v[136:139], v[152:155], v[64:67], v[136:139]\n"
      "ds_read_b64_tr_b16 a[18:19], v41 offset:20480\n"
      "v_mfma_f32_16x16x32_bf16 v[140:143], v[156:159], v[64:67], v[140:143]\n"
      "ds_read_b64_tr_b16 a[20:21], v41 offset:18432\n"
      "s_waitcnt vmcnt(6)\n"
      "v_mfma_f32_16x16x32_bf16 v[128:131], v[160:163], v[68:71], v[128:131]\n"
      "s_mov_b32 s13, 8192\n"
      "buffer_load_dwordx4 v[96:99], v29, s[36:39], s13 offen offset:0 sc1\n"
      "v_mfma_f32_16x16x32_bf16 v[132:135], v[164:167], v[68:71], v[132:135]\n"
      "ds_read_b64_tr_b16 a[22:23], v41 offset:22528\n"
      "v_mfma_f32_16x16x32_bf16 v[136:139], v[168:171], v[68:71], v[136:139]\n"
      "ds_read_b64_tr_b16 a[24:25], v41 offset:24576\n"
      "v_mfma_f32_16x16x32_bf16 v[140:143], v[172:175], v[68:71], v[140:143]\n"
      "ds_read_b64_tr_b16 a[26:27], v41 offset:28672\n"
      "s_waitcnt vmcnt(6)\n"
      "v_mfma_f32_16x16x32_bf16 v[128:131], v[176:179], v[72:75], v[128:131]\n"
      "buffer_load_dwordx4 v[100:103], v29, s[36:39], s13 offen offset:1024 sc1\n"
      "v_mfma_f32_16x16x32_bf16 v[132:135], v[180:183], v[72:75], v[132:135]\n"
      "ds_read_b64_tr_b16 a[28:29], v41 offset:26624\n"
      "v_mfma_f32_16x16x32_bf16 v[136:139], v[184:187], v[72:75], v[136:139]\n"
      "ds_read_b64_tr_b16 a[30:31], v41 offset:30720\n"
      "v_mfma_f32_16x16x32_bf16 v[140:143], v[188:191], v[72:75], v[140:143]\n"
      "ds_read_b64_tr_b16 a[32:33], v41 offset:32768\n"
      "s_waitcnt vmcnt(6)\n"
      "v_mfma_f32_16x16x32_bf16 v[128:131], v[192:195], v[76:79], v[128:131]\n"
      "buffer_load_dwordx4 v[104:107], v29, s[36:39], s13 offen offset:2048 sc1\n"
      "v_mfma_f32_16x16x32_bf16 v[132:135], v[196:199], v[76:79], v[132:135]\n"
      "ds_read_b64_tr_b16 a[34:35], v41 offset:36864\n"
      "v_mfma_f32_16x16x32_bf16 v[136:139], v[200:203], v[76:79], v[136:139]\n"
      "ds_read_b64_tr_b16 a[36:37], v41 offset:34816\n"
      "v_mfma_f32_16x16x32_bf16 v[140:143], v[204:207], v[76:79], v[140:143]\n"
      "ds_read_b64_tr_b16 a[38:39], v41 offset:38912\n"
      "s_waitcnt vmcnt(6)\n"
      "v_mfma_f32_16x16x32_bf16 v[128:131], v[208:211], v[80:83], v[128:131]\n"
      "buffer_load_dwordx4 v[108:111], v29, s[36:39], s13 offen offset:3072 sc1\n"
      "v_mfma_f32_16x16x32_bf16 v[132:135], v[212:215], v[80:83], v[132:135]\n"
      "ds_read_b64_tr_b16 a[40:41], v41 offset:40960\n"
      "v_mfma_f32_16x16x32_bf16 v[136:139], v[216:219], v[80:83], v[136:139]\n"
      "ds_read_b64_tr_b16 a[42:43], v41 offset:45056\n"
      "v_mfma_f32_16x16x32_bf16 v[140:143], v[220:223], v[80:83], v[140:143]\n"
      "ds_read_b64_tr_b16 a[44:45], v41 offset:43008\n"
      "s_waitcnt vmcnt(6)\n"
      "v_mfma_f32_16x16x32_bf16 v[128:131], v[224:227], v[84:87], v[128:131]\n"
      "s_mov_b32 s13, 12288\n"
      "buffer_load_dwordx4 v[112:115], v29, s[36:39], s13 offen offset:0 sc1\n"
      "v_mfma_f32_16x16x32_bf16 v[132:135], v[228:231], v[84:87], v[132:135]\n"
      "ds_read_b64_tr_b16 a[46:47], v41 offset:47104\n"
      "v_mfma_f32_16x16x32_bf16 v[136:139], v[232:235], v[84:87], v[136:139]\n"
      "ds_read_b64_tr_b16 a[48:49], v42 offset:0\n"
      "v_mfma_f32_16x16x32_bf16 v[140:143], v[236:239], v[84:87], v[140:143]\n"
      "ds_read_b64_tr_b16 a[50:51], v42 offset:4096\n"
      "s_waitcnt vmcnt(6)\n"
      "v_mfma_f32_16x16x32_bf16 v[128:131], v[240:243], v[88:91], v[128:131]\n"
      "buffer_load_dwordx4 v[116:119], v29, s[36:39], s13 offen offset:1024 sc1\n"
      "v_mfma_f32_16x16x32_bf16 v[132:135], v[244:247], v[88:91], v[132:135]\n"
      "ds_read_b64_tr_b16 a[52:53], v42 offset:2048\n"
      "v_mfma_f32_16x16x32_bf16 v[136:139], v[248:251], v[88:91], v[136:139]\n"
      "ds_read_b64_tr_b16 a[54:55], v42 offset:6144\n"
      "v_mfma_f32_16x16x32_bf16 v[140:143], v[252:255], v[88:91], v[140:143]\n"
      "ds_read_b64_tr_b16 a[56:57], v42 offset:8192\n"
      "s_waitcnt vmcnt(6)\n"
      "v_mfma_f32_16x16x32_bf16 v[128:131], a[112:115], v[92:95], v[128:131]\n"
      "buffer_load_dwordx4 v[120:123], v29, s[36:39], s13 offen offset:2048 sc1\n"
      "v_mfma_f32_16x16x32_bf16 v[132:135], a[116:119], v[92:95], v[132:135]\n"
      "ds_read_b64_tr_b16 a[58:59], v42 offset:12288\n"
      "v_mfma_f32_16x16x32_bf16 v[136:139], a[120:123], v[92:95], v[136:139]\n"
      "ds_read_b64_tr_b16 a[60:61], v42 offset:10240\n"
      "v_mfma_f32_16x16x32_bf16 v[140:143], a[124:127], v[92:95], v[140:143]\n"
      "ds_read_b64_tr_b16 a[62:63], v42 offset:14336\n"
      "s_waitcnt vmcnt(6)\n"
      "v_mfma_f32_16x16x32_bf16 v[128:131], a[128:131], v[96:99], v[128:131]\n"
      "buffer_load_dwordx4 v[124:127], v29, s[36:39], s13 offen offset:3072 sc1\n"
      "v_mfma_f32_16x16x32_bf16 v[132:135], a[132:135], v[96:99], v[132:135]\n"
      "ds_read_b64_tr_b16 a[64:65], v42 offset:16384\n"
      "v_mfma_f32_16x16x32_bf16 v[136:139], a[136:139], v[96:99], v[136:139]\n"
      "ds_read_b64_tr_b16 a[66:67], v42 offset:20480\n"
      "v_mfma_f32_16x16x32_bf16 v[140:143], a[140:143], v[96:99], v[140:143]\n"
      "ds_read_b64_tr_b16 a[68:69], v42 offset:18432\n"
      "s_waitcnt vmcnt(6)\n"
      "v_mfma_f32_16x16x32_bf16 v[128:131], a[144:147], v[100:103], v[128:131]\n"
      "s_mov_b32 m0, s52\n"
      "s_mov_b32 s66, 1261568\n"
      "buffer_load_dwordx4 v5, s[20:23], s66 offen lds\n"
      "v_mfma_f32_16x16x32_bf16 v[132:135], a[148:151], v[100:103], v[132:135]\n"
      "ds_read_b64_tr_b16 a[70:71], v42 offset:22528\n"
      "v_mfma_f32_16x16x32_bf16 v[136:139], a[152:155], v[100:103], v[136:139]\n"
      "ds_read_b64_tr_b16 a[72:73], v42 offset:24576\n"
      "v_mfma_f32_16x16x32_bf16 v[140:143], a[156:159], v[100:103], v[140:143]\n"
      "ds_read_b64_tr_b16 a[74:75], v42 offset:28672\n"
      "s_waitcnt vmcnt(6)\n"
      "v_mfma_f32_16x16x32_bf16 v[128:131], a[160:163], v[104:107], v[128:131]\n"
      "s_add_u32 m0, s52, 1024\n"
      "s_mov_b32 s66, 1275904\n"
      "buffer_load_dwordx4 v5, s[20:23], s66 offen lds\n"
      "v_mfma_f32_16x16x32_bf16 v[132:135], a[164:167], v[104:107], v[132:135]\n"
      "ds_read_b64_tr_b16 a[76:77], v42 offset:26624\n"
      "v_mfma_f32_16x16x32_bf16 v[136:139], a[168:171], v[104:107], v[136:139]\n"
      "ds_read_b64_tr_b16 a[78:79], v42 offset:30720\n"
      "v_mfma_f32_16x16x32_bf16 v[140:143], a[172:175], v[104:107], v[140:143]\n"
      "ds_read_b64_tr_b16 a[80:81], v42 offset:32768\n"
      "s_waitcnt vmcnt(6)\n"
      "v_mfma_f32_16x16x32_bf16 v[128:131], a[176:179], v[108:111], v[128:131]\n"
      "s_add_u32 m0, s52, 8192\n"
      "s_mov_b32 s66, 1376256\n"
      "buffer_load_dwordx4 v5, s[20:23], s66 offen lds\n"
      "v_mfma_f32_16x16x32_bf16 v[132:135], a[180:183], v[108:111], v[132:135]\n"
      "ds_read_b64_tr_b16 a[82:83], v42 offset:36864\n"
      "v_mfma_f32_16x16x32_bf16 v[136:139], a[184:187], v[108:111], v[136:139]\n"
      "ds_read_b64_tr_b16 a[84:85], v42 offset:34816\n"
      "v_mfma_f32_16x16x32_bf16 v[140:143], a[188:191], v[108:111], v[140:143]\n"
      "ds_read_b64_tr_b16 a[86:87], v42 offset:38912\n"
      "s_waitcnt vmcnt(6)\n"
      "v_mfma_f32_16x16x32_bf16 v[128:131], a[192:195], v[112:115], v[128:131]\n"
      "s_add_u32 m0, s52, 9216\n"
      "s_mov_b32 s66, 1390592\n"
      "buffer_load_dwordx4 v5, s[20:23], s66 offen lds\n"
      "v_mfma_f32_16x16x32_bf16 v[132:135], a[196:199], v[112:115], v[132:135]\n"
      "v_mfma_f32_16x16x32_bf16 v[136:139], a[200:203], v[112:115], v[136:139]\n"
      "v_mfma_f32_16x16x32_bf16 v[140:143], a[204:207], v[112:115], v[140:143]\n"
      "s_waitcnt vmcnt(6)\n"
      "v_mfma_f32_16x16x32_bf16 v[128:131], a[208:211], v[116:119], v[128:131]\n"
      "s_add_u32 m0, s52, 16384\n"
      "s_mov_b32 s66, 1490944\n"
      "buffer_load_dwordx4 v5, s[20:23], s66 offen lds\n"
      "v_mfma_f32_16x16x32_bf16 v[132:135], a[212:215], v[116:119], v[132:135]\n"
      "v_mfma_f32_16x16x32_bf16 v[136:139], a[216:219], v[116:119], v[136:139]\n"
      "v_mfma_f32_16x16x32_bf16 v[140:143], a[220:223], v[116:119], v[140:143]\n"
      "s_waitcnt vmcnt(6)\n"
      "v_mfma_f32_16x16x32_bf16 v[128:131], a[224:227], v[120:123], v[128:131]\n"
      "s_add_u32 m0, s52, 17408\n"
      "s_mov_b32 s66, 1505280\n"
      "buffer_load_dwordx4 v5, s[20:23], s66 offen lds\n"
      "v_mfma_f32_16x16x32_bf16 v[132:135], a[228:231], v[120:123], v[132:135]\n"
      "v_mfma_f32_16x16x32_bf16 v[136:139], a[232:235], v[120:123], v[136:139]\n"
      "v_mfma_f32_16x16x32_bf16 v[140:143], a[236:239], v[120:123], v[140:143]\n"
      "s_waitcnt vmcnt(6)\n"
      "v_mfma_f32_16x16x32_bf16 v[128:131], a[240:243], v[124:127], v[128:131]\n"
      "v_mfma_f32_16x16x32_bf16 v[132:135], a[244:247], v[124:127], v[132:135]\n"
      "v_mfma_f32_16x16x32_bf16 v[136:139], a[248:251], v[124:127], v[136:139]\n"
      "v_mfma_f32_16x16x32_bf16 v[140:143], a[252:255], v[124:127], v[140:143]\n"
      "buffer_store_dword v7, v3, s[36:39], 0 offen sc1\n"
      "s_nop 3\n"
      "v_cvt_pk_bf16_f32 v248, v128, v129\n"
      "v_cvt_pk_bf16_f32 v249, v130, v131\n"
      "v_cvt_pk_bf16_f32 v250, v132, v133\n"
      "v_cvt_pk_bf16_f32 v251, v134, v135\n"
      "buffer_store_dwordx4 v[248:251], v38, s[36:39], 0 offen offset:0 sc1\n"
      "v_cvt_pk_bf16_f32 v252, v136, v137\n"
      "v_cvt_pk_bf16_f32 v253, v138, v139\n"
      "v_cvt_pk_bf16_f32 v254, v140, v141\n"
      "v_cvt_pk_bf16_f32 v255, v142, v143\n"
      "buffer_store_dwordx4 v[252:255], v38, s[36:39], 0 offen offset:2048 sc1\n"
      "v_pk_mul_f32 v[14:15], v[128:129], v[128:129]\n"
      "v_pk_fma_f32 v[14:15], v[130:131], v[130:131], v[14:15]\n"
      "v_pk_add_f32 v[128:129], v[128:129], v[130:131]\n"
      "v_pk_fma_f32 v[14:15], v[132:133], v[132:133], v[14:15]\n"
      "v_pk_add_f32 v[128:129], v[128:129], v[132:133]\n"
      "v_pk_fma_f32 v[14:15], v[134:135], v[134:135], v[14:15]\n"
      "v_pk_add_f32 v[128:129], v[128:129], v[134:135]\n"
      "v_pk_fma_f32 v[14:15], v[136:137], v[136:137], v[14:15]\n"
      "v_pk_add_f32 v[128:129], v[128:129], v[136:137]\n"
      "v_pk_fma_f32 v[14:15], v[138:139], v[138:139], v[14:15]\n"
      "v_pk_add_f32 v[128:129], v[128:129], v[138:139]\n"
      "v_pk_fma_f32 v[14:15], v[140:141], v[140:141], v[14:15]\n"
      "v_pk_add_f32 v[128:129], v[128:129], v[140:141]\n"
      "v_pk_fma_f32 v[14:15], v[142:143], v[142:143], v[14:15]\n"
      "v_pk_add_f32 v[128:129], v[128:129], v[142:143]\n"
      "v_add_f32 v15, v14, v15\n"
      "v_add_f32 v14, v128, v129\n"
      "s_nop 1\n"
      "v_permlane32_swap_b32 v14, v15\n"
      "v_add_f32 v14, v14, v15\n"
      "v_mov_b32 v15, v14\n"
      "s_nop 1\n"
      "v_permlane16_swap_b32 v14, v15\n"
      "v_add_f32 v14, v14, v15\n"
      "buffer_store_dword v14, v39, s[36:39], 0 offen sc1\n"
      "s_lshl_b32 s14, s33, 13\n"
      "s_add_u32 s14, s14, 24576\n"
      "s_mul_i32 s13, s33, 22528\n"
      "s_add_u32 s13, s13, 57344\n"
      "s_mov_b32 m0, s14\n"
      "s_nop 0\n"
      "buffer_load_dwordx4 v43, s[24:27], 0 offen lds\n"
      "s_waitcnt lgkmcnt(14)\n"
      "v_mfma_f32_16x16x32_bf16 v[18:21], v[30:33], a[0:3], 0\n"
      "v_mfma_f32_16x16x32_bf16 v[18:21], v[30:33], a[4:7], v[18:21]\n"
      "s_mul_i32 s46, s68, 14\n"
      "s_add_u32 s46, s46, s69\n"
      "s_lshl_b32 s46, s46, 7\n"
      "s_add_u32 s46, s46, 118784\n"
      "s_add_u32 m0, s14, 1024\n"
      "s_mov_b32 s66, 7168\n"
      "buffer_load_dwordx4 v43, s[24:27], s66 offen lds\n"
      "v_mfma_f32_16x16x32_bf16 v[18:21], v[30:33], a[8:11], v[18:21]\n"
      "v_mfma_f32_16x16x32_bf16 v[18:21], v[30:33], a[12:15], v[18:21]\n"
      "v_lshl_add_u32 v4, v1, 2, s46\n"
      "v_add_u32 v41, -57344, v41\n"
      "v_add_u32 v24, s14, v44\n"
      "s_lshr_b32 s15, s33, 1\n"
      "s_add_u32 m0, s14, 2048\n"
      "s_mov_b32 s66, 14336\n"
      "buffer_load_dwordx4 v43, s[24:27], s66 offen lds\n"
      "v_mfma_f32_16x16x32_bf16 v[18:21], v[30:33], a[16:19], v[18:21]\n"
      "v_mfma_f32_16x16x32_bf16 v[18:21], v[30:33], a[20:23], v[18:21]\n"
      "s_mul_i32 s16, s15, 28672\n"
      "s_lshl_b32 s47, s51, 11\n"
      "s_add_u32 s16, s16, s47\n"
      "s_lshl2_add_u32 s46, s68, s15\n"
      "s_add_u32 m0, s14, 3072\n"
      "s_mov_b32 s66, 21504\n"
      "buffer_load_dwordx4 v43, s[24:27], s66 offen lds\n"
      "v_mfma_f32_16x16x32_bf16 v[18:21], v[30:33], a[24:27], v[18:21]\n"
      "v_mfma_f32_16x16x32_bf16 v[18:21], v[30:33], a[28:31], v[18:21]\n"
      "s_mul_i32 s46, s46, 28672\n"
      "s_add_u32 s46, s46, 3670016\n"
      "v_add_u32 v5, s46, v28\n"
      "v_add_u32 v6, -28672, v5\n"
      "s_waitcnt lgkmcnt(0)\n"
      "s_barrier\n"
      "v_mfma_f32_16x16x32_bf16 v[18:21], v[30:33], a[32:35], v[18:21]\n"
      "v_mfma_f32_16x16x32_bf16 v[18:21], v[30:33], a[36:39], v[18:21]\n"
      "s_lshl2_add_u32 s46, s68, s33\n"
      "s_lshl_b32 s46, s46, 11\n"
      "s_add_u32 s46, s46, 348160\n"
      "v_lshl_add_u32 v11, v1, 2, s46\n"
      "s_xor_b32 s48, s72, 0\n"
      "s_mul_i32 s11, s48, 114688\n"
      "s_lshl_b32 s12, s48, 12\n"
      "s_add_u32 s12, s12, s13\n"
      "s_mov_b32 m0, s12\n"
      "s_add_u32 s66, s11, 114688\n"
      "buffer_load_dwordx4 v43, s[24:27], s66 offen lds\n"
      "v_mfma_f32_16x16x32_bf16 v[18:21], v[30:33], a[40:43], v[18:21]\n"
      "v_mfma_f32_16x16x32_bf16 v[18:21], v[30:33], a[44:47], v[18:21]\n"
      "s_mul_i32 s46, s68, 458752\n"
      "s_lshl_b32 s47, s69, 13\n"
      "s_add_u32 s46, s46, s47\n"
      "s_add_u32 s46, s46, s52\n"
      "s_add_u32 m0, s12, 1024\n"
      "s_add_u32 s66, s11, 121856\n"
      "buffer_load_dwordx4 v43, s[24:27], s66 offen lds\n"
      "v_mfma_f32_16x16x32_bf16 v[18:21], v[30:33], a[48:51], v[18:21]\n"
      "v_mfma_f32_16x16x32_bf16 v[18:21], v[30:33], a[52:55], v[18:21]\n"
      "s_add_u32 s46, s46, 5767168\n"
      "v_add_u32 v13, s46, v28\n"
      "v_add_u32 v7, 114688, v40\n"
      "v_add_u32 v14, s13, v44\n"
      "s_add_u32 m0, s12, 2048\n"
      "s_add_u32 s66, s11, 129024\n"
      "buffer_load_dwordx4 v43, s[24:27], s66 offen lds\n"
      "v_mfma_f32_16x16x32_bf16 v[18:21], v[30:33], a[56:59], v[18:21]\n"
      "v_mfma_f32_16x16x32_bf16 v[18:21], v[30:33], a[60:63], v[18:21]\n"
      "v_add_u32 v15, 57344, v28\n"
      "s_mul_i32 s17, s33, 9216\n"
      "s_add_u32 s17, s17, 114688\n"
      "v_add_u32 v25, s17, v44\n"
      "s_add_u32 m0, s12, 3072\n"
      "s_add_u32 s66, s11, 136192\n"
      "buffer_load_dwordx4 v43, s[24:27], s66 offen lds\n"
      "v_mfma_f32_16x16x32_bf16 v[18:21], v[30:33], a[64:67], v[18:21]\n"
      "v_mfma_f32_16x16x32_bf16 v[18:21], v[30:33], a[68:71], v[18:21]\n"
      "v_add_u32 v29, 159744, v28\n"
      "v_add_u32 v26, s64, v29\n"
      "s_mov_b32 s18, 114688\n"
      "s_mov_b32 s19, 229376\n"
      "s_xor_b32 s48, s72, 1\n"
      "s_mul_i32 s11, s48, 114688\n"
      "s_lshl_b32 s12, s48, 12\n"
      "s_add_u32 s12, s12, s13\n"
      "s_mov_b32 m0, s12\n"
      "s_add_u32 s66, s11, 114688\n"
      "buffer_load_dwordx4 v43, s[24:27], s66 offen lds\n"
      "v_mfma_f32_16x16x32_bf16 v[18:21], v[30:33], a[72:75], v[18:21]\n"
      "v_mfma_f32_16x16x32_bf16 v[18:21], v[30:33], a[76:79], v[18:21]\n"
      "s_mov_b32 s34, 344064\n"
      "s_add_u32 m0, s12, 1024\n"
      "s_add_u32 s66, s11, 121856\n"
      "buffer_load_dwordx4 v43, s[24:27], s66 offen lds\n"
      "v_mfma_f32_16x16x32_bf16 v[18:21], v[30:33], a[80:83], v[18:21]\n"
      "v_mfma_f32_16x16x32_bf16 v[18:21], v[30:33], a[84:87], v[18:21]\n"
      "s_waitcnt vmcnt(10)\n"
      "s_barrier\n"
      "s_mov_b64 exec, s[8:9]\n"
      "buffer_atomic_swap v8, v40, s[36:39], 0 offen offset:0\n"
      "s_mov_b64 exec, -1\n"
      "s_add_u32 m0, s12, 2048\n"
      "s_add_u32 s66, s11, 129024\n"
      "buffer_load_dwordx4 v43, s[24:27], s66 offen lds\n"
      "ds_read_b64_tr_b16 a[88:89], v41 offset:0\n"
      "ds_read_b64_tr_b16 a[90:91], v41 offset:4096\n"
      "ds_read_b64_tr_b16 a[92:93], v41 offset:2048\n"
      "ds_read_b64_tr_b16 a[94:95], v41 offset:6144\n"
      "s_add_u32 m0, s12, 3072\n"
      "s_add_u32 s66, s11, 136192\n"
      "buffer_load_dwordx4 v43, s[24:27], s66 offen lds\n"
      "ds_read_b64_tr_b16 a[96:97], v41 offset:8192\n"
      "ds_read_b64_tr_b16 a[98:99], v41 offset:12288\n"
      "ds_read_b64_tr_b16 a[100:101], v41 offset:10240\n"
      "ds_read_b64_tr_b16 a[102:103], v41 offset:14336\n"
      "s_xor_b32 s48, s72, 2\n"
      "s_mul_i32 s11, s48, 114688\n"
      "s_lshl_b32 s12, s48, 12\n"
      "s_add_u32 s12, s12, s13\n"
      "s_mov_b32 m0, s12\n"
      "s_add_u32 s66, s11, 114688\n"
      "buffer_load_dwordx4 v43, s[24:27], s66 offen lds\n"
      "ds_read_b64_tr_b16 a[104:105], v41 offset:16384\n"
      "ds_read_b64_tr_b16 a[106:107], v41 offset:20480\n"
      "ds_read_b64_tr_b16 a[108:109], v41 offset:18432\n"
      "ds_read_b64_tr_b16 a[110:111], v41 offset:22528\n"
      "v_mov_b32 v3, v8\n"
      "s_mov_b64 exec, 0x3fff\n"
      "v_mov_b32 v3, 0\n"
      "buffer_atomic_or v3, v4, s[36:39], 0 offen offset:0 sc0\n"
      "s_mov_b64 exec, -1\n"
      "s_add_u32 m0, s12, 1024\n"
      "s_add_u32 s66, s11, 121856\n"
      "buffer_load_dwordx4 v43, s[24:27], s66 offen lds\n"
      "s_waitcnt vmcnt(14)\n"
      "ds_read_b64_tr_b16 a[112:113], v24 offset:0\n"
      "ds_read_b64_tr_b16 a[114:115], v24 offset:1024\n"
      "ds_read_b64_tr_b16 a[116:117], v24 offset:256\n"
      "ds_read_b64_tr_b16 a[118:119], v24 offset:1280\n"
      "s_add_u32 m0, s12, 2048\n"
      "s_add_u32 s66, s11, 129024\n"
      "buffer_load_dwordx4 v43, s[24:27], s66 offen lds\n"
      "s_waitcnt vmcnt(13)\n"
      "ds_read_b64_tr_b16 a[120:121], v24 offset:2048\n"
      "ds_read_b64_tr_b16 a[122:123], v24 offset:3072\n"
      "ds_read_b64_tr_b16 a[124:125], v24 offset:2304\n"
      "ds_read_b64_tr_b16 a[126:127], v24 offset:3328\n"
      "s_add_u32 m0, s12, 3072\n"
      "s_add_u32 s66, s11, 136192\n"
      "buffer_load_dwordx4 v43, s[24:27], s66 offen lds\n"
      "s_waitcnt lgkmcnt(14)\n"
      "v_mfma_f32_16x16x32_bf16 v[18:21], v[30:33], a[88:91], v[18:21]\n"
      "v_mfma_f32_16x16x32_bf16 v[18:21], v[30:33], a[92:95], v[18:21]\n"
      "v_mfma_f32_16x16x32_bf16 v[18:21], v[30:33], a[96:99], v[18:21]\n"
      "s_waitcnt lgkmcnt(12)\n"
      "v_mfma_f32_16x16x32_bf16 v[18:21], v[30:33], a[100:103], v[18:21]\n"
      "s_xor_b32 s48, s72, 3\n"
      "s_mul_i32 s11, s48, 114688\n"
      "s_lshl_b32 s12, s48, 12\n"
      "s_add_u32 s12, s12, s13\n"
      "s_mov_b32 m0, s12\n"
      "s_add_u32 s66, s11, 114688\n"
      "buffer_load_dwordx4 v43, s[24:27], s66 offen lds\n"
      "s_waitcnt lgkmcnt(10)\n"
      "v_mfma_f32_16x16x32_bf16 v[18:21], v[30:33], a[104:107], v[18:21]\n"
      "s_waitcnt lgkmcnt(8)\n"
      "v_mfma_f32_16x16x32_bf16 v[18:21], v[30:33], a[108:111], v[18:21]\n"
      "s_waitcnt lgkmcnt(0)\n"
      "s_barrier\n"
      "v_lshlrev_b32 v2, 16, v27\n"
      "s_add_u32 m0, s12, 1024\n"
      "s_add_u32 s66, s11, 121856\n"
      "buffer_load_dwordx4 v43, s[24:27], s66 offen lds\n"
      "s_nop 1\n"
      "v_cvt_pk_bf16_f32 v47, v18, v2\n"
      "v_lshlrev_b32 v2, 16, v47\n"
      "v_sub_f32 v49, v18, v2\n"
      "v_cvt_pk_bf16_f32 v46, v2, v49\n"
      "s_add_u32 m0, s12, 2048\n"
      "s_add_u32 s66, s11, 129024\n"
      "buffer_load_dwordx4 v43, s[24:27], s66 offen lds\n"
      "v_cndmask_b32 v46, v46, 0, s[74:75]\n"
      "v_cndmask_b32 v47, v47, 0, s[74:75]\n"
      "v_cndmask_b32 v48, v27, 0, s[74:75]\n"
      "v_mov_b32 v49, 0\n"
      "s_add_u32 m0, s12, 3072\n"
      "s_add_u32 s66, s11, 136192\n"
      "buffer_load_dwordx4 v43, s[24:27], s66 offen lds\n"
      "s_add_u32 m0, s13, 16384\n"
      "s_mov_b32 s66, 573440\n"
      "buffer_load_dwordx4 v43, s[24:27], s66 offen lds\n"
      "s_add_u32 m0, s13, 17408\n"
      "s_mov_b32 s66, 580608\n"
      "buffer_load_dwordx4 v43, s[24:27], s66 offen lds\n"
      "s_add_u32 m0, s13, 18432\n"
      "s_mov_b32 s66, 587776\n"
      "buffer_load_dwordx4 v43, s[24:27], s66 offen lds\n"
      "s_add_u32 m0, s13, 19456\n"
      "s_mov_b32 s66, 594944\n"
      "buffer_load_dwordx4 v43, s[24:27], s66 offen lds\n"
      "s_add_u32 m0, s13, 20480\n"
      "s_mov_b32 s66, 688128\n"
      "buffer_load_dwordx4 v43, s[24:27], s66 offen lds\n"
      "s_add_u32 m0, s13, 21504\n"
      "s_mov_b32 s66, 695296\n"
      "buffer_load_dwordx4 v43, s[24:27], s66 offen lds\n"
      "s_waitcnt vmcnt(13)\n"
      "v_cmp_ne_u32 vcc, s73, v3\n"
      "s_nop 0\n"
      "s_cbranch_vccnz .Lk8_p3slow_%=\n"
      ".Lk9_p3back_%=:\n"
      "s_mov_b32 m0, s16\n"
      "s_nop 0\n"
      "buffer_load_dwordx4 v5, s[36:39], m0 offen sc1 lds\n"
      "buffer_load_dwordx4 v5, s[36:39], m0 offen offset:1024 sc1 lds\n"
      "s_add_u32 m0, s16, 4096\n"
      "s_nop 0\n"
      "buffer_load_dwordx4 v5, s[36:39], m0 offen sc1 lds\n"
      "buffer_load_dwordx4 v5, s[36:39], m0 offen offset:1024 sc1 lds\n"
      "s_add_u32 m0, s16, 8192\n"
      "s_nop 0\n"
      "buffer_load_dwordx4 v5, s[36:39], m0 offen sc1 lds\n"
      "buffer_load_dwordx4 v5, s[36:39], m0 offen offset:1024 sc1 lds\n"
      "s_add_u32 m0, s16, 12288\n"
      "s_nop 0\n"
      "buffer_load_dwordx4 v5, s[36:39], m0 offen sc1 lds\n"
      "buffer_load_dwordx4 v5, s[36:39], m0 offen offset:1024 sc1 lds\n"
      "s_waitcnt vmcnt(14)\n"
      "ds_read_b64_tr_b16 a[128:129], v14 offset:0\n"
      "ds_read_b64_tr_b16 a[130:131], v14 offset:1024\n"
      "ds_read_b64_tr_b16 a[132:133], v14 offset:256\n"
      "ds_read_b64_tr_b16 a[134:135], v14 offset:1280\n"
      "ds_read_b64_tr_b16 a[136:137], v14 offset:2048\n"
      "ds_read_b64_tr_b16 a[138:139], v14 offset:3072\n"
      "ds_read_b64_tr_b16 a[140:141], v14 offset:2304\n"
      "ds_read_b64_tr_b16 a[142:143], v14 offset:3328\n"
      "ds_read_b64_tr_b16 a[144:145], v14 offset:4096\n"
      "ds_read_b64_tr_b16 a[146:147], v14 offset:5120\n"
      "ds_read_b64_tr_b16 a[148:149], v14 offset:4352\n"
      "ds_read_b64_tr_b16 a[150:151], v14 offset:5376\n"
      "ds_read_b64_tr_b16 a[152:153], v14 offset:6144\n"
      "ds_read_b64_tr_b16 a[154:155], v14 offset:7168\n"
      "ds_read_b64_tr_b16 a[156:157], v14 offset:6400\n"
      "ds_read_b64_tr_b16 a[158:159], v14 offset:7424\n"
      "ds_read_b64_tr_b16 a[160:161], v14 offset:8192\n"
      "ds_read_b64_tr_b16 a[162:163], v14 offset:9216\n"
      "ds_read_b64_tr_b16 a[164:165], v14 offset:8448\n"
      "ds_read_b64_tr_b16 a[166:167], v14 offset:9472\n"
      "ds_read_b64_tr_b16 a[168:169], v14 offset:10240\n"
      "ds_read_b64_tr_b16 a[170:171], v14 offset:11264\n"
      "ds_read_b64_tr_b16 a[172:173], v14 offset:10496\n"
      "ds_read_b64_tr_b16 a[174:175], v14 offset:11520\n"
      "ds_read_b64_tr_b16 a[176:177], v14 offset:12288\n"
      "ds_read_b64_tr_b16 a[178:179], v14 offset:13312\n"
      "ds_read_b64_tr_b16 a[180:181], v14 offset:12544\n"
      "ds_read_b64_tr_b16 a[182:183], v14 offset:13568\n"
      "ds_read_b64_tr_b16 a[184:185], v14 offset:14336\n"
      "ds_read_b64_tr_b16 a[186:187], v14 offset:15360\n"
      "ds_read_b64_tr_b16 a[188:189], v14 offset:14592\n"
      "ds_read_b64_tr_b16 a[190:191], v14 offset:15616\n"
      "s_waitcnt vmcnt(12)\n"
      "ds_read_b64_tr_b16 a[192:193], v14 offset:16384\n"
      "ds_read_b64_tr_b16 a[194:195], v14 offset:17408\n"
      "ds_read_b64_tr_b16 a[196:197], v14 offset:16640\n"
      "ds_read_b64_tr_b16 a[198:199], v14 offset:17664\n"
      "s_waitcnt vmcnt(10)\n"
      "ds_read_b64_tr_b16 a[200:201], v14 offset:18432\n"
      "ds_read_b64_tr_b16 a[202:203], v14 offset:19456\n"
      "ds_read_b64_tr_b16 a[204:205], v14 offset:18688\n"
      "ds_read_b64_tr_b16 a[206:207], v14 offset:19712\n"
      "s_waitcnt vmcnt(8)\n"
      "ds_read_b64_tr_b16 a[208:209], v14 offset:20480\n"
      "ds_read_b64_tr_b16 a[210:211], v14 offset:21504\n"
      "ds_read_b64_tr_b16 a[212:213], v14 offset:20736\n"
      "ds_read_b64_tr_b16 a[214:215], v14 offset:21760\n"
      "s_waitcnt lgkmcnt(0)\n"
      "s_waitcnt vmcnt(6)\n"
      "s_barrier\n"
      "ds_read_b128 v[216:219], v28 offset:0\n"
      "ds_read_b128 v[220:223], v28 offset:28672\n"
      "ds_read_b128 v[224:227], v28 offset:1024\n"
      "ds_read_b128 v[228:231], v28 offset:29696\n"
      "ds_read_b128 v[232:235], v28 offset:2048\n"
      "ds_read_b128 v[236:239], v28 offset:30720\n"
      "s_waitcnt lgkmcnt(4)\n"
      "v_mfma_f32_32x32x16_bf16 v[152:167], a[0:3], v[216:219], 0\n"
      "ds_read_b128 v[240:243], v28 offset:3072\n"
      "ds_read_b128 v[244:247], v28 offset:31744\n"
      "s_and_b32 s48, s70, 1\n"
      "s_mul_i32 s46, s48, 28672\n"
      "s_lshl1_add_u32 s47, s71, s15\n"
      "s_lshl_b32 s47, s47, 11\n"
      "v_mfma_f32_32x32x16_bf16 v[168:183], a[0:3], v[220:223], 0\n"
      "s_add_u32 m0, s16, 16384\n"
      "s_nop 0\n"
      "buffer_load_dwordx4 v5, s[36:39], m0 offen sc1 lds\n"
      "s_add_u32 s46, s46, s47\n"
      "s_lshl3_add_u32 s46, s51, s46\n"
      "s_waitcnt lgkmcnt(4)\n"
      "v_mfma_f32_32x32x16_bf16 v[152:167], a[4:7], v[224:227], v[152:167]\n"
      "s_waitcnt vmcnt(5)\n"
      "s_barrier\n"
      "ds_read_b128 v[216:219], v28 offset:4096\n"
      "ds_read_b128 v[220:223], v28 offset:32768\n"
      "v_add_u32 v14, s46, v28\n"
      "s_lshl_b32 s46, s48, 11\n"
      "v_mfma_f32_32x32x16_bf16 v[168:183], a[4:7], v[228:231], v[168:183]\n"
      "buffer_load_dwordx4 v5, s[36:39], m0 offen offset:1024 sc1 lds\n"
      "v_add_u32 v24, s46, v29\n"
      "v_add_u32 v4, 114688, v4\n"
      "s_mul_i32 s46, s70, 229376\n"
      "s_add_u32 s46, s46, s52\n"
      "s_waitcnt lgkmcnt(4)\n"
      "v_mfma_f32_32x32x16_bf16 v[152:167], a[8:11], v[232:235], v[152:167]\n"
      "ds_read_b128 v[224:227], v28 offset:5120\n"
      "ds_read_b128 v[228:231], v28 offset:33792\n"
      "s_add_u32 s46, s46, 5767168\n"
      "v_add_u32 v32, s46, v28\n"
      "v_add_u32 v33, 114688, v32\n"
      "v_add_u32 v38, s50, v44\n"
      "v_mfma_f32_32x32x16_bf16 v[168:183], a[8:11], v[236:239], v[168:183]\n"
      "s_add_u32 m0, s16, 20480\n"
      "s_nop 0\n"
      "buffer_load_dwordx4 v5, s[36:39], m0 offen sc1 lds\n"
      "v_mul_u32_u24 v27, 896, v16\n"
      "v_lshl_add_u32 v27, v17, 4, v27\n"
      "s_waitcnt lgkmcnt(4)\n"
      "v_mfma_f32_32x32x16_bf16 v[152:167], a[12:15], v[240:243], v[152:167]\n"
      "ds_read_b128 v[232:235], v28 offset:6144\n"
      "ds_read_b128 v[236:239], v28 offset:34816\n"
      "s_mul_i32 s46, s70, 57344\n"
      "s_lshl2_add_u32 s47, s71, s33\n"
      "s_lshl_b32 s47, s47, 5\n"
      "s_add_u32 s46, s46, s47\n"
      "v_mfma_f32_32x32x16_bf16 v[168:183], a[12:15], v[244:247], v[168:183]\n"
      "buffer_load_dwordx4 v5, s[36:39], m0 offen offset:1024 sc1 lds\n"
      "v_add_u32 v27, s46, v27\n"
      "v_add_u32 v39, s50, v28\n"
      "s_mov_b32 s11, 28672\n"
      "s_xor_b32 s46, s33, 1\n"
      "s_waitcnt lgkmcnt(4)\n"
      "v_mfma_f32_32x32x16_bf16 v[152:167], a[16:19], v[216:219], v[152:167]\n"
      "ds_read_b128 v[240:243], v28 offset:7168\n"
      "ds_read_b128 v[244:247], v28 offset:35840\n"
      "s_mul_i32 s46, s46, 14336\n"
      "v_add_u32 v40, s46, v28\n"
      "s_xor_b32 s46, s33, 2\n"
      "s_mul_i32 s46, s46, 14336\n"
      "v_mfma_f32_32x32x16_bf16 v[168:183], a[16:19], v[220:223], v[168:183]\n"
      "s_add_u32 m0, s16, 24576\n"
      "s_nop 0\n"
      "buffer_load_dwordx4 v5, s[36:39], m0 offen sc1 lds\n"
      "v_add_u32 v41, s46, v28\n"
      "s_xor_b32 s46, s33, 3\n"
      "s_waitcnt lgkmcnt(4)\n"
      "v_mfma_f32_32x32x16_bf16 v[152:167], a[20:23], v[224:227], v[152:167]\n"
      "s_waitcnt vmcnt(5)\n"
      "s_barrier\n"
      "ds_read_b128 v[216:219], v28 offset:8192\n"
      "ds_read_b128 v[220:223], v28 offset:36864\n"
      "s_mul_i32 s46, s46, 14336\n"
      "v_add_u32 v42, s46, v28\n"
      "v_mfma_f32_32x32x16_bf16 v[168:183], a[20:23], v[228:231], v[168:183]\n"
      "buffer_load_dwordx4 v5, s[36:39], m0 offen offset:1024 sc1 lds\n"
      "s_waitcnt lgkmcnt(4)\n"
      "v_mfma_f32_32x32x16_bf16 v[152:167], a[24:27], v[232:235], v[152:167]\n"
      "ds_read_b128 v[224:227], v28 offset:9216\n"
      "ds_read_b128 v[228:231], v28 offset:37888\n"
      "v_mfma_f32_32x32x16_bf16 v[168:183], a[24:27], v[236:239], v[168:183]\n"
      "buffer_load_dword v248, v11, s[36:39], 0 offen offset:0 sc1\n"
      "buffer_load_dword v249, v11, s[36:39], 0 offen offset:256 sc1\n"
      "buffer_load_dword v250, v11, s[36:39], 0 offen offset:512 sc1\n"
      "buffer_load_dword v251, v11, s[36:39], 0 offen offset:768 sc1\n"
      "s_waitcnt lgkmcnt(4)\n"
      "v_mfma_f32_32x32x16_bf16 v[152:167], a[28:31], v[240:243], v[152:167]\n"
      "ds_read_b128 v[232:235], v28 offset:10240\n"
      "ds_read_b128 v[236:239], v28 offset:38912\n"
      "v_mfma_f32_32x32x16_bf16 v[168:183], a[28:31], v[244:247], v[168:183]\n"
      "buffer_load_dword v252, v11, s[36:39], 0 offen offset:1024 sc1\n"
      "buffer_load_dword v253, v11, s[36:39], 0 offen offset:1280 sc1\n"
      "buffer_load_dword v254, v11, s[36:39], 0 offen offset:1536 sc1\n"
      "s_waitcnt lgkmcnt(4)\n"
      "v_mfma_f32_32x32x16_bf16 v[152:167], a[32:35], v[216:219], v[152:167]\n"
      "ds_read_b128 v[240:243], v28 offset:11264\n"
      "ds_read_b128 v[244:247], v28 offset:39936\n"
      "v_mfma_f32_32x32x16_bf16 v[168:183], a[32:35], v[220:223], v[168:183]\n"
      "s_add_u32 m0, s16, 57344\n"
      "s_nop 0\n"
      "buffer_load_dwordx4 v6, s[36:39], m0 offen sc1 lds\n"
      "s_waitcnt lgkmcnt(4)\n"
      "v_mfma_f32_32x32x16_bf16 v[152:167], a[36:39], v[224:227], v[152:167]\n"
      "ds_read_b128 v[216:219], v28 offset:12288\n"
      "ds_read_b128 v[220:223], v28 offset:40960\n"
      "v_mfma_f32_32x32x16_bf16 v[168:183], a[36:39], v[228:231], v[168:183]\n"
      "buffer_load_dwordx4 v6, s[36:39], m0 offen offset:1024 sc1 lds\n"
      "s_waitcnt lgkmcnt(4)\n"
      "v_mfma_f32_32x32x16_bf16 v[152:167], a[40:43], v[232:235], v[152:167]\n"
      "ds_read_b128 v[224:227], v28 offset:13312\n"
      "ds_read_b128 v[228:231], v28 offset:41984\n"
      "v_mfma_f32_32x32x16_bf16 v[168:183], a[40:43], v[236:239], v[168:183]\n"
      "s_add_u32 m0, s16, 61440\n"
      "s_nop 0\n"
      "buffer_load_dwordx4 v6, s[36:39], m0 offen sc1 lds\n"
      "s_waitcnt lgkmcnt(4)\n"
      "v_mfma_f32_32x32x16_bf16 v[152:167], a[44:47], v[240:243], v[152:167]\n"
      "ds_read_b128 v[232:235], v28 offset:14336\n"
      "ds_read_b128 v[236:239], v28 offset:43008\n"
      "v_mfma_f32_32x32x16_bf16 v[168:183], a[44:47], v[244:247], v[168:183]\n"
      "buffer_load_dwordx4 v6, s[36:39], m0 offen offset:1024 sc1 lds\n"
      "s_waitcnt lgkmcnt(4)\n"
      "v_mfma_f32_32x32x16_bf16 v[152:167], a[48:51], v[216:219], v[152:167]\n"
      "ds_read_b128 v[240:243], v28 offset:15360\n"
      "ds_read_b128 v[244:247], v28 offset:44032\n"
      "v_mfma_f32_32x32x16_bf16 v[168:183], a[48:51], v[220:223], v[168:183]\n"
      "s_add_u32 m0, s16, 65536\n"
      "s_nop 0\n"
      "buffer_load_dwordx4 v6, s[36:39], m0 offen sc1 lds\n"
      "s_waitcnt lgkmcnt(4)\n"
      "v_mfma_f32_32x32x16_bf16 v[152:167], a[52:55], v[224:227], v[152:167]\n"
      "s_waitcnt vmcnt(16)\n"
      "s_barrier\n"
      "ds_read_b128 v[216:219], v28 offset:16384\n"
      "ds_read_b128 v[220:223], v28 offset:45056\n"
      "v_mfma_f32_32x32x16_bf16 v[168:183], a[52:55], v[228:231], v[168:183]\n"
      "buffer_load_dwordx4 v6, s[36:39], m0 offen offset:1024 sc1 lds\n"
      "s_waitcnt lgkmcnt(4)\n"
      "v_mfma_f32_32x32x16_bf16 v[152:167], a[56:59], v[232:235], v[152:167]\n"
      "ds_read_b128 v[224:227], v28 offset:17408\n"
      "ds_read_b128 v[228:231], v28 offset:46080\n"
      "v_mfma_f32_32x32x16_bf16 v[168:183], a[56:59], v[236:239], v[168:183]\n"
      "s_add_u32 m0, s16, 69632\n"
      "s_nop 0\n"
      "buffer_load_dwordx4 v6, s[36:39], m0 offen sc1 lds\n"
      "s_waitcnt lgkmcnt(4)\n"
      "v_mfma_f32_32x32x16_bf16 v[152:167], a[60:63], v[240:243], v[152:167]\n"
      "ds_read_b128 v[232:235], v28 offset:18432\n"
      "ds_read_b128 v[236:239], v28 offset:47104\n"
      "v_mfma_f32_32x32x16_bf16 v[168:183], a[60:63], v[244:247], v[168:183]\n"
      "buffer_load_dwordx4 v6, s[36:39], m0 offen offset:1024 sc1 lds\n"
      "s_waitcnt lgkmcnt(4)\n"
      "v_mfma_f32_32x32x16_bf16 v[152:167], a[64:67], v[216:219], v[152:167]\n"
      "ds_read_b128 v[240:243], v28 offset:19456\n"
      "ds_read_b128 v[244:247], v28 offset:48128\n"
      "v_mfma_f32_32x32x16_bf16 v[168:183], a[64:67], v[220:223], v[168:183]\n"
      "s_add_u32 m0, s16, 73728\n"
      "s_nop 0\n"
      "buffer_load_dwordx4 v6, s[36:39], m0 offen sc1 lds\n"
      "s_waitcnt lgkmcnt(4)\n"
      "v_mfma_f32_32x32x16_bf16 v[152:167], a[68:71], v[224:227], v[152:167]\n"
      "s_waitcnt vmcnt(16)\n"
      "s_barrier\n"
      "ds_read_b128 v[216:219], v28 offset:20480\n"
      "ds_read_b128 v[220:223], v28 offset:49152\n"
      "v_mfma_f32_32x32x16_bf16 v[168:183], a[68:71], v[228:231], v[168:183]\n"
      "buffer_load_dwordx4 v6, s[36:39], m0 offen offset:1024 sc1 lds\n"
      "s_waitcnt lgkmcnt(4)\n"
      "v_mfma_f32_32x32x16_bf16 v[152:167], a[72:75], v[232:235], v[152:167]\n"
      "ds_read_b128 v[224:227], v28 offset:21504\n"
      "ds_read_b128 v[228:231], v28 offset:50176\n"
      "s_waitcnt vmcnt(10)\n"
      "v_add_f32 v248, v248, v249\n"
      "v_add_f32 v250, v250, v251\n"
      "v_add_f32 v252, v252, v253\n"
      "v_mfma_f32_32x32x16_bf16 v[168:183], a[72:75], v[236:239], v[168:183]\n"
      "s_add_u32 m0, s16, 77824\n"
      "s_nop 0\n"
      "buffer_load_dwordx4 v6, s[36:39], m0 offen sc1 lds\n"
      "v_add_f32 v248, v248, v254\n"
      "v_add_f32 v250, v250, v252\n"
      "s_waitcnt lgkmcnt(4)\n"
      "v_mfma_f32_32x32x16_bf16 v[152:167], a[76:79], v[240:243], v[152:167]\n"
      "ds_read_b128 v[232:235], v28 offset:22528\n"
      "ds_read_b128 v[236:239], v28 offset:51200\n"
      "v_add_f32 v248, v248, v250\n"
      "v_mov_b32 v249, v248\n"
      "s_nop 1\n"
      "v_permlane32_swap_b32 v248, v249\n"
      "v_mfma_f32_32x32x16_bf16 v[168:183], a[76:79], v[244:247], v[168:183]\n"
      "buffer_load_dwordx4 v6, s[36:39], m0 offen offset:1024 sc1 lds\n"
      "v_mul_f32 v248, 0xbb124925, v248\n"
      "v_mul_f32 v249, 0x3b124925, v249\n"
      "v_fma_f32 v249, -v248, v248, v249\n"
      "v_max_f32 v249, 0, v249\n"
      "s_waitcnt lgkmcnt(4)\n"
      "v_mfma_f32_32x32x16_bf16 v[152:167], a[80:83], v[216:219], v[152:167]\n"
      "ds_read_b128 v[240:243], v28 offset:23552\n"
      "ds_read_b128 v[244:247], v28 offset:52224\n"
      "v_add_f32 v249, 0x3727c5ac, v249\n"
      "v_rcp_f32 v251, v249\n"
      "v_sqrt_f32 v249, v249\n"
      "v_cvt_pk_bf16_f32 v18, v248, v248\n"
      "v_mfma_f32_32x32x16_bf16 v[168:183], a[80:83], v[220:223], v[168:183]\n"
      "s_add_u32 m0, s16, 81920\n"
      "s_nop 0\n"
      "buffer_load_dwordx4 v6, s[36:39], m0 offen sc1 lds\n"
      "v_and_b32 v21, 0xffffff7f, v251\n"
      "v_lshlrev_b32 v250, 16, v18\n"
      "s_waitcnt lgkmcnt(4)\n"
      "v_mfma_f32_32x32x16_bf16 v[152:167], a[84:87], v[224:227], v[152:167]\n"
      "ds_read_b128 v[216:219], v28 offset:24576\n"
      "ds_read_b128 v[220:223], v28 offset:53248\n"
      "v_sub_f32 v250, v248, v250\n"
      "v_cvt_pk_bf16_f32 v19, v250, v249\n"
      "v_and_b32 v250, 0xffff0000, v19\n"
      "v_sub_f32 v250, v249, v250\n"
      "v_mfma_f32_32x32x16_bf16 v[168:183], a[84:87], v[228:231], v[168:183]\n"
      "buffer_load_dwordx4 v6, s[36:39], m0 offen offset:1024 sc1 lds\n"
      "v_cvt_pk_bf16_f32 v20, v250, 0\n"
      "ds_write_b128 v26, v[18:21]\n"
      "s_waitcnt lgkmcnt(5)\n"
      "v_mfma_f32_32x32x16_bf16 v[152:167], a[88:91], v[232:235], v[152:167]\n"
      "ds_read_b128 v[224:227], v28 offset:25600\n"
      "ds_read_b128 v[228:231], v28 offset:54272\n"
      "v_mfma_f32_32x32x16_bf16 v[168:183], a[88:91], v[236:239], v[168:183]\n"
      "s_mov_b32 m0, s17\n"
      "s_mov_b32 s66, 702464\n"
      "buffer_load_dwordx4 v43, s[24:27], s66 offen lds\n"
      "s_waitcnt lgkmcnt(5)\n"
      "v_mfma_f32_32x32x16_bf16 v[152:167], a[92:95], v[240:243], v[152:167]\n"
      "ds_read_b128 v[232:235], v28 offset:26624\n"
      "ds_read_b128 v[236:239], v28 offset:55296\n"
      "v_mfma_f32_32x32x16_bf16 v[168:183], a[92:95], v[244:247], v[168:183]\n"
      "s_add_u32 m0, s17, 1024\n"
      "s_mov_b32 s66, 709632\n"
      "buffer_load_dwordx4 v43, s[24:27], s66 offen lds\n"
      "s_waitcnt lgkmcnt(5)\n"
      "v_mfma_f32_32x32x16_bf16 v[152:167], a[96:99], v[216:219], v[152:167]\n"
      "ds_read_b128 v[240:243], v28 offset:27648\n"
      "ds_read_b128 v[244:247], v28 offset:56320\n"
      "v_mfma_f32_32x32x16_bf16 v[168:183], a[96:99], v[220:223], v[168:183]\n"
      "s_add_u32 m0, s17, 2048\n"
      "s_mov_b32 s66, 802816\n"
      "buffer_load_dwordx4 v43, s[24:27], s66 offen lds\n"
      "s_waitcnt lgkmcnt(4)\n"
      "v_mfma_f32_32x32x16_bf16 v[152:167], a[100:103], v[224:227], v[152:167]\n"
      "s_waitcnt vmcnt(13)\n"
      "s_barrier\n"
      "ds_read_b128 v[216:219], v15 offset:0\n"
      "ds_read_b128 v[220:223], v15 offset:28672\n"
      "v_mfma_f32_32x32x16_bf16 v[168:183], a[100:103], v[228:231], v[168:183]\n"
      "s_add_u32 m0, s17, 3072\n"
      "s_mov_b32 s66, 809984\n"
      "buffer_load_dwordx4 v43, s[24:27], s66 offen lds\n"
      "s_waitcnt lgkmcnt(4)\n"
      "v_mfma_f32_32x32x16_bf16 v[152:167], a[104:107], v[232:235], v[152:167]\n"
      "ds_read_b128 v[224:227], v15 offset:1024\n"
      "ds_read_b128 v[228:231], v15 offset:29696\n"
      "v_mfma_f32_32x32x16_bf16 v[168:183], a[104:107], v[236:239], v[168:183]\n"
      "s_add_u32 m0, s17, 4096\n"
      "s_mov_b32 s66, 817152\n"
      "buffer_load_dwordx4 v43, s[24:27], s66 offen lds\n"
      "ds_read_b128 v[136:139], v29 offset:0\n"
      "ds_read_b128 v[140:143], v29 offset:1024\n"
      "ds_read_b128 v[144:147], v29 offset:2048\n"
      "ds_read_b128 v[148:151], v29 offset:3072\n"
      "s_waitcnt lgkmcnt(8)\n"
      "v_mfma_f32_32x32x16_bf16 v[152:167], a[108:111], v[240:243], v[152:167]\n"
      "ds_read_b128 v[232:235], v15 offset:2048\n"
      "ds_read_b128 v[236:239], v15 offset:30720\n"
      "v_mfma_f32_32x32x16_bf16 v[168:183], a[108:111], v[244:247], v[168:183]\n"
      "s_add_u32 m0, s17, 5120\n"
      "s_mov_b32 s66, 824320\n"
      "buffer_load_dwordx4 v43, s[24:27], s66 offen lds\n"
      "ds_read_b64 v[128:129], v14 offset:0\n"
      "ds_read_b32 v30, v24 offset:12\n"
      "ds_read_b64 v[130:131], v14 offset:1024\n"
      "ds_read_b32 v31, v24 offset:1036\n"
      "s_waitcnt lgkmcnt(12)\n"
      "v_mfma_f32_32x32x16_bf16 v[184:199], a[0:3], v[216:219], 0\n"
      "ds_read_b128 v[240:243], v15 offset:3072\n"
      "ds_read_b128 v[244:247], v15 offset:31744\n"
      "v_mfma_f32_32x32x16_bf16 v[200:215], a[0:3], v[220:223], 0\n"
      "s_add_u32 m0, s17, 6144\n"
      "s_mov_b32 s66, 917504\n"
      "buffer_load_dwordx4 v43, s[24:27], s66 offen lds\n"
      "s_waitcnt lgkmcnt(11)\n"
      "v_mfma_f32_32x32x16_bf16 v[152:167], v[46:49], v[136:139], v[152:167]\n"
      "s_waitcnt lgkmcnt(9)\n"
      "v_mfma_f32_32x32x16_bf16 v[168:183], v[46:49], v[144:147], v[168:183]\n"
      "v_mfma_f32_32x32x16_bf16 v[184:199], v[46:49], v[140:143], v[184:199]\n"
      "s_add_u32 m0, s17, 7168\n"
      "s_mov_b32 s66, 924672\n"
      "buffer_load_dwordx4 v43, s[24:27], s66 offen lds\n"
      "s_waitcnt lgkmcnt(8)\n"
      "v_mfma_f32_32x32x16_bf16 v[200:215], v[46:49], v[148:151], v[200:215]\n"
      "v_mfma_f32_32x32x16_bf16 v[184:199], a[4:7], v[224:227], v[184:199]\n"
      "ds_read_b128 v[216:219], v15 offset:4096\n"
      "ds_read_b128 v[220:223], v15 offset:32768\n"
      "s_nop 0\n"
      "v_mul_f32_e64 v152, v152, abs(v152)\n"
      "v_mul_f32_e64 v153, v153, abs(v153)\n"
      "v_mul_f32_e64 v154, v154, abs(v154)\n"
      "v_mfma_f32_32x32x16_bf16 v[200:215], a[4:7], v[228:231], v[200:215]\n"
      "v_mul_f32_e64 v155, v155, abs(v155)\n"
      "v_mul_f32_e64 v156, v156, abs(v156)\n"
      "v_mul_f32_e64 v157, v157, abs(v157)\n"
      "v_mul_f32_e64 v158, v158, abs(v158)\n"
      "v_mul_f32_e64 v159, v159, abs(v159)\n"
      "s_waitcnt lgkmcnt(8)\n"
      "v_mfma_f32_32x32x16_bf16 v[184:199], a[8:11], v[232:235], v[184:199]\n"
      "ds_read_b128 v[224:227], v15 offset:5120\n"
      "ds_read_b128 v[228:231], v15 offset:33792\n"
      "v_cvt_pk_bf16_f32 v152, v152, v153\n"
      "v_cvt_pk_bf16_f32 v153, v154, v155\n"
      "v_cvt_pk_bf16_f32 v154, v156, v157\n"
      "v_cvt_pk_bf16_f32 v155, v158, v159\n"
      "v_mfma_f32_32x32x16_bf16 v[200:215], a[8:11], v[236:239], v[200:215]\n"
      "v_pk_max_i16 v152, v152, 0\n"
      "v_pk_max_i16 v153, v153, 0\n"
      "v_pk_max_i16 v154, v154, 0\n"
      "v_pk_max_i16 v155, v155, 0\n"
      "buffer_store_dwordx4 v[152:155], v13, s[36:39], 0 offen offset:0 sc1\n"
      "s_waitcnt lgkmcnt(4)\n"
      "v_mfma_f32_32x32x16_bf16 v[184:199], a[12:15], v[240:243], v[184:199]\n"
      "ds_read_b128 v[232:235], v15 offset:6144\n"
      "ds_read_b128 v[236:239], v15 offset:34816\n"
      "v_mul_f32_e64 v160, v160, abs(v160)\n"
      "v_mul_f32_e64 v161, v161, abs(v161)\n"
      "v_mul_f32_e64 v162, v162, abs(v162)\n"
      "v_mul_f32_e64 v163, v163, abs(v163)\n"
      "v_mfma_f32_32x32x16_bf16 v[200:215], a[12:15], v[244:247], v[200:215]\n"
      "v_mul_f32_e64 v164, v164, abs(v164)\n"
      "v_mul_f32_e64 v165, v165, abs(v165)\n"
      "v_mul_f32_e64 v166, v166, abs(v166)\n"
      "v_mul_f32_e64 v167, v167, abs(v167)\n"
      "v_cvt_pk_bf16_f32 v160, v160, v161\n"
      "s_waitcnt lgkmcnt(4)\n"
      "v_mfma_f32_32x32x16_bf16 v[184:199], a[16:19], v[216:219], v[184:199]\n"
      "ds_read_b128 v[240:243], v15 offset:7168\n"
      "ds_read_b128 v[244:247], v15 offset:35840\n"
      "v_cvt_pk_bf16_f32 v161, v162, v163\n"
      "v_cvt_pk_bf16_f32 v162, v164, v165\n"
      "v_cvt_pk_bf16_f32 v163, v166, v167\n"
      "v_pk_max_i16 v160, v160, 0\n"
      "v_mfma_f32_32x32x16_bf16 v[200:215], a[16:19], v[220:223], v[200:215]\n"
      "v_pk_max_i16 v161, v161, 0\n"
      "v_pk_max_i16 v162, v162, 0\n"
      "v_pk_max_i16 v163, v163, 0\n"
      "buffer_store_dwordx4 v[160:163], v13, s[36:39], 0 offen offset:1024 sc1\n"
      "v_mul_f32_e64 v168, v168, abs(v168)\n"
      "s_waitcnt lgkmcnt(4)\n"
      "v_mfma_f32_32x32x16_bf16 v[184:199], a[20:23], v[224:227], v[184:199]\n"
      "s_waitcnt vmcnt(10)\n"
      "s_barrier\n"
      "ds_read_b128 v[216:219], v15 offset:8192\n"
      "ds_read_b128 v[220:223], v15 offset:36864\n"
      "v_mul_f32_e64 v169, v169, abs(v169)\n"
      "v_mul_f32_e64 v170, v170, abs(v170)\n"
      "v_mfma_f32_32x32x16_bf16 v[200:215], a[20:23], v[228:231], v[200:215]\n"
      "s_mov_b32 m0, s50\n"
      "s_mov_b32 s66, 931840\n"
      "buffer_load_dwordx4 v43, s[24:27], s66 offen lds\n"
      "v_mul_f32_e64 v171, v171, abs(v171)\n"
      "v_mul_f32_e64 v172, v172, abs(v172)\n"
      "s_waitcnt lgkmcnt(4)\n"
      "v_mfma_f32_32x32x16_bf16 v[184:199], a[24:27], v[232:235], v[184:199]\n"
      "ds_read_b128 v[224:227], v15 offset:9216\n"
      "ds_read_b128 v[228:231], v15 offset:37888\n"
      "v_mul_f32_e64 v173, v173, abs(v173)\n"
      "v_mul_f32_e64 v174, v174, abs(v174)\n"
      "v_mul_f32_e64 v175, v175, abs(v175)\n"
      "v_cvt_pk_bf16_f32 v168, v168, v169\n"
      "v_mfma_f32_32x32x16_bf16 v[200:215], a[24:27], v[236:239], v[200:215]\n"
      "s_add_u32 m0, s50, 1024\n"
      "s_mov_b32 s66, 939008\n"
      "buffer_load_dwordx4 v43, s[24:27], s66 offen lds\n"
      "v_cvt_pk_bf16_f32 v169, v170, v171\n"
      "v_cvt_pk_bf16_f32 v170, v172, v173\n"
      "s_waitcnt lgkmcnt(4)\n"
      "v_mfma_f32_32x32x16_bf16 v[184:199], a[28:31], v[240:243], v[184:199]\n"
      "ds_read_b128 v[232:235], v15 offset:10240\n"
      "ds_read_b128 v[236:239], v15 offset:38912\n"
      "v_cvt_pk_bf16_f32 v171, v174, v175\n"
      "v_pk_max_i16 v168, v168, 0\n"
      "v_pk_max_i16 v169, v169, 0\n"
      "v_pk_max_i16 v170, v170, 0\n"
      "v_mfma_f32_32x32x16_bf16 v[200:215], a[28:31], v[244:247], v[200:215]\n"
      "s_add_u32 m0, s50, 2048\n"
      "s_mov_b32 s66, 1032192\n"
      "buffer_load_dwordx4 v43, s[24:27], s66 offen lds\n"
      "v_pk_max_i16 v171, v171, 0\n"
      "buffer_store_dwordx4 v[168:171], v13, s[36:39], s19 offen offset:0 sc1\n"
      "s_waitcnt lgkmcnt(4)\n"
      "v_mfma_f32_32x32x16_bf16 v[184:199], a[32:35], v[216:219], v[184:199]\n"
      "ds_read_b128 v[240:243], v15 offset:11264\n"
      "ds_read_b128 v[244:247], v15 offset:39936\n"
      "v_mul_f32_e64 v176, v176, abs(v176)\n"
      "v_mul_f32_e64 v177, v177, abs(v177)\n"
      "v_mul_f32_e64 v178, v178, abs(v178)\n"
      "v_mul_f32_e64 v179, v179, abs(v179)\n"
      "v_mfma_f32_32x32x16_bf16 v[200:215], a[32:35], v[220:223], v[200:215]\n"
      "s_add_u32 m0, s50, 3072\n"
      "s_mov_b32 s66, 1039360\n"
      "buffer_load_dwordx4 v43, s[24:27], s66 offen lds\n"
      "v_mul_f32_e64 v180, v180, abs(v180)\n"
      "v_mul_f32_e64 v181, v181, abs(v181)\n"
      "s_waitcnt lgkmcnt(4)\n"
      "v_mfma_f32_32x32x16_bf16 v[184:199], a[36:39], v[224:227], v[184:199]\n"
      "ds_read_b128 v[216:219], v15 offset:12288\n"
      "ds_read_b128 v[220:223], v15 offset:40960\n"
      "v_mul_f32_e64 v182, v182, abs(v182)\n"
      "v_mul_f32_e64 v183, v183, abs(v183)\n"
      "v_cvt_pk_bf16_f32 v176, v176, v177\n"
      "v_cvt_pk_bf16_f32 v177, v178, v179\n"
      "v_mfma_f32_32x32x16_bf16 v[200:215], a[36:39], v[228:231], v[200:215]\n"
      "s_add_u32 m0, s50, 4096\n"
      "s_mov_b32 s66, 1046528\n"
      "buffer_load_dwordx4 v43, s[24:27], s66 offen lds\n"
      "v_cvt_pk_bf16_f32 v178, v180, v181\n"
      "v_cvt_pk_bf16_f32 v179, v182, v183\n"
      "s_waitcnt lgkmcnt(4)\n"
      "v_mfma_f32_32x32x16_bf16 v[184:199], a[40:43], v[232:235], v[184:199]\n"
      "ds_read_b128 v[224:227], v15 offset:13312\n"
      "ds_read_b128 v[228:231], v15 offset:41984\n"
      "v_pk_max_i16 v176, v176, 0\n"
      "v_pk_max_i16 v177, v177, 0\n"
      "v_pk_max_i16 v178, v178, 0\n"
      "v_pk_max_i16 v179, v179, 0\n"
      "v_mfma_f32_32x32x16_bf16 v[200:215], a[40:43], v[236:239], v[200:215]\n"
      "s_add_u32 m0, s50, 5120\n"
      "s_mov_b32 s66, 1053696\n"
      "buffer_load_dwordx4 v43, s[24:27], s66 offen lds\n"
      "buffer_store_dwordx4 v[176:179], v13, s[36:39], s19 offen offset:1024 sc1\n"
      "s_waitcnt lgkmcnt(4)\n"
      "v_mfma_f32_32x32x16_bf16 v[184:199], a[44:47], v[240:243], v[184:199]\n"
      "ds_read_b128 v[232:235], v15 offset:14336\n"
      "ds_read_b128 v[236:239], v15 offset:43008\n"
      "v_mfma_f32_32x32x16_bf16 v[200:215], a[44:47], v[244:247], v[200:215]\n"
      "s_add_u32 m0, s50, 6144\n"
      "s_mov_b32 s66, 1146880\n"
      "buffer_load_dwordx4 v43, s[24:27], s66 offen lds\n"
      "s_waitcnt lgkmcnt(4)\n"
      "v_mfma_f32_32x32x16_bf16 v[184:199], a[48:51], v[216:219], v[184:199]\n"
      "ds_read_b128 v[240:243], v15 offset:15360\n"
      "ds_read_b128 v[244:247], v15 offset:44032\n"
      "v_mfma_f32_32x32x16_bf16 v[200:215], a[48:51], v[220:223], v[200:215]\n"
      "s_add_u32 m0, s50, 7168\n"
      "s_mov_b32 s66, 1154048\n"
      "buffer_load_dwordx4 v43, s[24:27], s66 offen lds\n"
      "s_waitcnt lgkmcnt(4)\n"
      "v_mfma_f32_32x32x16_bf16 v[184:199], a[52:55], v[224:227], v[184:199]\n"
      "ds_read_b128 v[216:219], v15 offset:16384\n"
      "ds_read_b128 v[220:223], v15 offset:45056\n"
      "s_waitcnt vmcnt(18)\n"
      "ds_read_b64_tr_b16 a[216:217], v25 offset:0\n"
      "ds_read_b64_tr_b16 a[218:219], v25 offset:1024\n"
      "ds_read_b64_tr_b16 a[220:221], v25 offset:256\n"
      "v_mfma_f32_32x32x16_bf16 v[200:215], a[52:55], v[228:231], v[200:215]\n"
      "s_add_u32 m0, s50, 8192\n"
      "s_mov_b32 s66, 1161216\n"
      "buffer_load_dwordx4 v43, s[24:27], s66 offen lds\n"
      "ds_read_b64_tr_b16 a[222:223], v25 offset:1280\n"
      "s_waitcnt vmcnt(17)\n"
      "ds_read_b64_tr_b16 a[224:225], v25 offset:2048\n"
      "s_waitcnt lgkmcnt(9)\n"
      "v_mfma_f32_32x32x16_bf16 v[184:199], a[56:59], v[232:235], v[184:199]\n"
      "ds_read_b128 v[224:227], v15 offset:17408\n"
      "ds_read_b128 v[228:231], v15 offset:46080\n"
      "ds_read_b64_tr_b16 a[226:227], v25 offset:3072\n"
      "ds_read_b64_tr_b16 a[228:229], v25 offset:2304\n"
      "ds_read_b64_tr_b16 a[230:231], v25 offset:3328\n"
      "s_waitcnt vmcnt(15)\n"
      "ds_read_b64_tr_b16 a[232:233], v25 offset:4096\n"
      "v_mfma_f32_32x32x16_bf16 v[200:215], a[56:59], v[236:239], v[200:215]\n"
      "s_add_u32 m0, s50, 9216\n"
      "s_mov_b32 s66, 1168384\n"
      "buffer_load_dwordx4 v43, s[24:27], s66 offen lds\n"
      "ds_read_b64_tr_b16 a[234:235], v25 offset:5120\n"
      "ds_read_b64_tr_b16 a[236:237], v25 offset:4352\n"
      "s_waitcnt lgkmcnt(14)\n"
      "v_mfma_f32_32x32x16_bf16 v[184:199], a[60:63], v[240:243], v[184:199]\n"
      "ds_read_b128 v[232:235], v15 offset:18432\n"
      "ds_read_b128 v[236:239], v15 offset:47104\n"
      "ds_read_b64_tr_b16 a[238:239], v25 offset:5376\n"
      "s_waitcnt vmcnt(14)\n"
      "ds_read_b64_tr_b16 a[240:241], v25 offset:6144\n"
      "ds_read_b64_tr_b16 a[242:243], v25 offset:7168\n"
      "v_mfma_f32_32x32x16_bf16 v[200:215], a[60:63], v[244:247], v[200:215]\n"
      "s_add_u32 m0, s50, 10240\n"
      "s_mov_b32 s66, 1261568\n"
      "buffer_load_dwordx4 v43, s[24:27], s66 offen lds\n"
      "ds_read_b64_tr_b16 a[244:245], v25 offset:6400\n"
      "ds_read_b64_tr_b16 a[246:247], v25 offset:7424\n"
      "s_waitcnt lgkmcnt(14)\n"
      "v_mfma_f32_32x32x16_bf16 v[184:199], a[64:67], v[216:219], v[184:199]\n"
      "ds_read_b128 v[240:243], v15 offset:19456\n"
      "ds_read_b128 v[244:247], v15 offset:48128\n"
      "v_mfma_f32_32x32x16_bf16 v[200:215], a[64:67], v[220:223], v[200:215]\n"
      "s_add_u32 m0, s50, 11264\n"
      "s_mov_b32 s66, 1268736\n"
      "buffer_load_dwordx4 v43, s[24:27], s66 offen lds\n"
      "ds_read_b64 v[132:133], v14 offset:57344\n"
      "ds_read_b64 v[134:135], v14 offset:58368\n"
      "s_waitcnt lgkmcnt(14)\n"
      "v_mfma_f32_32x32x16_bf16 v[184:199], a[68:71], v[224:227], v[184:199]\n"
      "ds_read_b128 v[216:219], v15 offset:20480\n"
      "ds_read_b128 v[220:223], v15 offset:49152\n"
      "v_mfma_f32_32x32x16_bf16 v[200:215], a[68:71], v[228:231], v[200:215]\n"
      "s_add_u32 m0, s50, 12288\n"
      "s_mov_b32 s66, 1275904\n"
      "buffer_load_dwordx4 v43, s[24:27], s66 offen lds\n"
      "s_waitcnt lgkmcnt(11)\n"
      "v_mfma_f32_32x32x16_bf16 v[184:199], a[72:75], v[232:235], v[184:199]\n"
      "ds_read_b128 v[224:227], v15 offset:21504\n"
      "ds_read_b128 v[228:231], v15 offset:50176\n"
      "v_mfma_f32_32x32x16_bf16 v[200:215], a[72:75], v[236:239], v[200:215]\n"
      "s_add_u32 m0, s50, 13312\n"
      "s_mov_b32 s66, 1283072\n"
      "buffer_load_dwordx4 v43, s[24:27], s66 offen lds\n"
      "s_waitcnt lgkmcnt(6)\n"
      "v_mfma_f32_32x32x16_bf16 v[184:199], a[76:79], v[240:243], v[184:199]\n"
      "ds_read_b128 v[232:235], v15 offset:22528\n"
      "ds_read_b128 v[236:239], v15 offset:51200\n"
      "v_mfma_f32_32x32x16_bf16 v[200:215], a[76:79], v[244:247], v[200:215]\n"
      "s_mov_b32 m0, s17\n"
      "s_mov_b32 s66, 1376256\n"
      "buffer_load_dwordx4 v43, s[24:27], s66 offen lds\n"
      "s_waitcnt lgkmcnt(4)\n"
      "v_mfma_f32_32x32x16_bf16 v[184:199], a[80:83], v[216:219], v[184:199]\n"
      "ds_read_b128 v[240:243], v15 offset:23552\n"
      "ds_read_b128 v[244:247], v15 offset:52224\n"
      "v_mfma_f32_32x32x16_bf16 v[200:215], a[80:83], v[220:223], v[200:215]\n"
      "s_add_u32 m0, s17, 1024\n"
      "s_mov_b32 s66, 1383424\n"
      "buffer_load_dwordx4 v43, s[24:27], s66 offen lds\n"
      "s_waitcnt lgkmcnt(4)\n"
      "v_mfma_f32_32x32x16_bf16 v[184:199], a[84:87], v[224:227], v[184:199]\n"
      "ds_read_b128 v[216:219], v15 offset:24576\n"
      "ds_read_b128 v[220:223], v15 offset:53248\n"
      "v_mfma_f32_32x32x16_bf16 v[200:215], a[84:87], v[228:231], v[200:215]\n"
      "s_add_u32 m0, s17, 2048\n"
      "s_mov_b32 s66, 1390592\n"
      "buffer_load_dwordx4 v43, s[24:27], s66 offen lds\n"
      "s_waitcnt lgkmcnt(4)\n"
      "v_mfma_f32_32x32x16_bf16 v[184:199], a[88:91], v[232:235], v[184:199]\n"
      "ds_read_b128 v[224:227], v15 offset:25600\n"
      "ds_read_b128 v[228:231], v15 offset:54272\n"
      "v_mfma_f32_32x32x16_bf16 v[200:215], a[88:91], v[236:239], v[200:215]\n"
      "s_add_u32 m0, s17, 3072\n"
      "s_mov_b32 s66, 1397760\n"
      "buffer_load_dwordx4 v43, s[24:27], s66 offen lds\n"
      "s_waitcnt vmcnt(12)\n"
      "s_barrier\n"
      "s_mov_b64 exec, s[8:9]\n"
      "buffer_atomic_swap v8, v7, s[36:39], 0 offen offset:0\n"
      "s_mov_b64 exec, -1\n"
      "s_waitcnt lgkmcnt(4)\n"
      "v_mfma_f32_32x32x16_bf16 v[184:199], a[92:95], v[240:243], v[184:199]\n"
      "ds_read_b128 v[232:235], v15 offset:26624\n"
      "ds_read_b128 v[236:239], v15 offset:55296\n"
      "v_mfma_f32_32x32x16_bf16 v[200:215], a[92:95], v[244:247], v[200:215]\n"
      "s_add_u32 m0, s17, 4096\n"
      "s_mov_b32 s66, 1490944\n"
      "buffer_load_dwordx4 v43, s[24:27], s66 offen lds\n"
      "s_waitcnt lgkmcnt(4)\n"
      "v_mfma_f32_32x32x16_bf16 v[184:199], a[96:99], v[216:219], v[184:199]\n"
      "ds_read_b128 v[240:243], v15 offset:27648\n"
      "ds_read_b128 v[244:247], v15 offset:56320\n"
      "v_mfma_f32_32x32x16_bf16 v[200:215], a[96:99], v[220:223], v[200:215]\n"
      "s_add_u32 m0, s17, 5120\n"
      "s_mov_b32 s66, 1498112\n"
      "buffer_load_dwordx4 v43, s[24:27], s66 offen lds\n"
      "s_waitcnt lgkmcnt(4)\n"
      "v_mfma_f32_32x32x16_bf16 v[184:199], a[100:103], v[224:227], v[184:199]\n"
      "v_mfma_f32_32x32x16_bf16 v[200:215], a[100:103], v[228:231], v[200:215]\n"
      "s_add_u32 m0, s17, 6144\n"
      "s_mov_b32 s66, 1505280\n"
      "buffer_load_dwordx4 v43, s[24:27], s66 offen lds\n"
      "s_waitcnt lgkmcnt(2)\n"
      "v_mfma_f32_32x32x16_bf16 v[184:199], a[104:107], v[232:235], v[184:199]\n"
      "v_mfma_f32_32x32x16_bf16 v[200:215], a[104:107], v[236:239], v[200:215]\n"
      "s_add_u32 m0, s17, 7168\n"
      "s_mov_b32 s66, 1512448\n"
      "buffer_load_dwordx4 v43, s[24:27], s66 offen lds\n"
      "s_waitcnt lgkmcnt(0)\n"
      "v_mfma_f32_32x32x16_bf16 v[184:199], a[108:111], v[240:243], v[184:199]\n"
      "v_mfma_f32_32x32x16_bf16 v[200:215], a[108:111], v[244:247], v[200:215]\n"
      "v_mov_b32 v3, v8\n"
      "s_mov_b64 exec, 0x3fff\n"
      "v_mov_b32 v3, 0\n"
      "buffer_atomic_or v3, v4, s[36:39], 0 offen offset:0 sc0\n"
      "s_mov_b64 exec, -1\n"
      "s_nop 5\n"
      "v_mul_f32_e64 v184, v184, abs(v184)\n"
      "v_mul_f32_e64 v185, v185, abs(v185)\n"
      "v_mul_f32_e64 v186, v186, abs(v186)\n"
      "v_mul_f32_e64 v187, v187, abs(v187)\n"
      "v_mul_f32_e64 v188, v188, abs(v188)\n"
      "v_mul_f32_e64 v189, v189, abs(v189)\n"
      "v_mul_f32_e64 v190, v190, abs(v190)\n"
      "v_mul_f32_e64 v191, v191, abs(v191)\n"
      "v_cvt_pk_bf16_f32 v184, v184, v185\n"
      "v_cvt_pk_bf16_f32 v185, v186, v187\n"
      "v_cvt_pk_bf16_f32 v186, v188, v189\n"
      "v_cvt_pk_bf16_f32 v187, v190, v191\n"
      "v_pk_max_i16 v184, v184, 0\n"
      "v_pk_max_i16 v185, v185, 0\n"
      "v_pk_max_i16 v186, v186, 0\n"
      "v_pk_max_i16 v187, v187, 0\n"
      "buffer_store_dwordx4 v[184:187], v13, s[36:39], s18 offen offset:0 sc1\n"
      "v_mul_f32_e64 v192, v192, abs(v192)\n"
      "v_mul_f32_e64 v193, v193, abs(v193)\n"
      "v_mul_f32_e64 v194, v194, abs(v194)\n"
      "v_mul_f32_e64 v195, v195, abs(v195)\n"
      "v_mul_f32_e64 v196, v196, abs(v196)\n"
      "v_mul_f32_e64 v197, v197, abs(v197)\n"
      "v_mul_f32_e64 v198, v198, abs(v198)\n"
      "v_mul_f32_e64 v199, v199, abs(v199)\n"
      "v_cvt_pk_bf16_f32 v192, v192, v193\n"
      "v_cvt_pk_bf16_f32 v193, v194, v195\n"
      "v_cvt_pk_bf16_f32 v194, v196, v197\n"
      "v_cvt_pk_bf16_f32 v195, v198, v199\n"
      "v_pk_max_i16 v192, v192, 0\n"
      "v_pk_max_i16 v193, v193, 0\n"
      "v_pk_max_i16 v194, v194, 0\n"
      "v_pk_max_i16 v195, v195, 0\n"
      "buffer_store_dwordx4 v[192:195], v13, s[36:39], s18 offen offset:1024 sc1\n"
      "v_mul_f32_e64 v200, v200, abs(v200)\n"
      "v_mul_f32_e64 v201, v201, abs(v201)\n"
      "v_mul_f32_e64 v202, v202, abs(v202)\n"
      "v_mul_f32_e64 v203, v203, abs(v203)\n"
      "v_mul_f32_e64 v204, v204, abs(v204)\n"
      "v_mul_f32_e64 v205, v205, abs(v205)\n"
      "v_mul_f32_e64 v206, v206, abs(v206)\n"
      "v_mul_f32_e64 v207, v207, abs(v207)\n"
      "v_cvt_pk_bf16_f32 v200, v200, v201\n"
      "v_cvt_pk_bf16_f32 v201, v202, v203\n"
      "v_cvt_pk_bf16_f32 v202, v204, v205\n"
      "v_cvt_pk_bf16_f32 v203, v206, v207\n"
      "v_pk_max_i16 v200, v200, 0\n"
      "v_pk_max_i16 v201, v201, 0\n"
      "v_pk_max_i16 v202, v202, 0\n"
      "v_pk_max_i16 v203, v203, 0\n"
      "buffer_store_dwordx4 v[200:203], v13, s[36:39], s34 offen offset:0 sc1\n"
      "v_mul_f32_e64 v208, v208, abs(v208)\n"
      "v_mul_f32_e64 v209, v209, abs(v209)\n"
      "v_mul_f32_e64 v210, v210, abs(v210)\n"
      "v_mul_f32_e64 v211, v211, abs(v211)\n"
      "v_mul_f32_e64 v212, v212, abs(v212)\n"
      "v_mul_f32_e64 v213, v213, abs(v213)\n"
      "v_mul_f32_e64 v214, v214, abs(v214)\n"
      "v_mul_f32_e64 v215, v215, abs(v215)\n"
      "v_cvt_pk_bf16_f32 v208, v208, v209\n"
      "v_cvt_pk_bf16_f32 v209, v210, v211\n"
      "v_cvt_pk_bf16_f32 v210, v212, v213\n"
      "v_cvt_pk_bf16_f32 v211, v214, v215\n"
      "v_pk_max_i16 v208, v208, 0\n"
      "v_pk_max_i16 v209, v209, 0\n"
      "v_pk_max_i16 v210, v210, 0\n"
      "v_pk_max_i16 v211, v211, 0\n"
      "buffer_store_dwordx4 v[208:211], v13, s[36:39], s34 offen offset:1024 sc1\n"
      "ds_read_b64_tr_b16 a[248:249], v38 offset:0\n"
      "ds_read_b64_tr_b16 a[250:251], v38 offset:1024\n"
      "ds_read_b64_tr_b16 a[252:253], v38 offset:256\n"
      "ds_read_b64_tr_b16 a[254:255], v38 offset:1280\n"
      "ds_read_b64_tr_b16 a[0:1], v38 offset:2048\n"
      "ds_read_b64_tr_b16 a[2:3], v38 offset:3072\n"
      "ds_read_b64_tr_b16 a[4:5], v38 offset:2304\n"
      "ds_read_b64_tr_b16 a[6:7], v38 offset:3328\n"
      "ds_read_b64_tr_b16 a[8:9], v38 offset:4096\n"
      "ds_read_b64_tr_b16 a[10:11], v38 offset:5120\n"
      "ds_read_b64_tr_b16 a[12:13], v38 offset:4352\n"
      "ds_read_b64_tr_b16 a[14:15], v38 offset:5376\n"
      "s_waitcnt vmcnt(20)\n"
      "ds_read_b64_tr_b16 a[16:17], v38 offset:6144\n"
      "ds_read_b64_tr_b16 a[18:19], v38 offset:7168\n"
      "ds_read_b64_tr_b16 a[20:21], v38 offset:6400\n"
      "ds_read_b64_tr_b16 a[22:23], v38 offset:7424\n"
      "s_waitcnt vmcnt(18)\n"
      "ds_read_b64_tr_b16 a[24:25], v38 offset:8192\n"
      "ds_read_b64_tr_b16 a[26:27], v38 offset:9216\n"
      "ds_read_b64_tr_b16 a[28:29], v38 offset:8448\n"
      "ds_read_b64_tr_b16 a[30:31], v38 offset:9472\n"
      "s_waitcnt vmcnt(4)\n"
      "v_cmp_ne_u32 vcc, s73, v3\n"
      "s_nop 0\n"
      "s_cbranch_vccnz .Lk10_p4slow_%=\n"
      ".Lk11_p4back_%=:\n"
      "buffer_load_dwordx4 v[56:59], v32, s[36:39], 0 offen offset:0 sc1\n"
      "ds_read_b64_tr_b16 a[32:33], v38 offset:10240\n"
      "ds_read_b64_tr_b16 a[34:35], v38 offset:11264\n"
      "ds_read_b64_tr_b16 a[36:37], v38 offset:10496\n"
      "ds_read_b64_tr_b16 a[38:39], v38 offset:11520\n"
      "buffer_load_dwordx4 v[60:63], v32, s[36:39], 0 offen offset:1024 sc1\n"
      "ds_read_b64_tr_b16 a[40:41], v38 offset:12288\n"
      "ds_read_b64_tr_b16 a[42:43], v38 offset:13312\n"
      "ds_read_b64_tr_b16 a[44:45], v38 offset:12544\n"
      "ds_read_b64_tr_b16 a[46:47], v38 offset:13568\n"
      "s_mov_b32 s66, 8192\n"
      "buffer_load_dwordx4 v[64:67], v32, s[36:39], s66 offen offset:0 sc1\n"
      "ds_read_b64_tr_b16 a[48:49], v25 offset:0\n"
      "ds_read_b64_tr_b16 a[50:51], v25 offset:1024\n"
      "ds_read_b64_tr_b16 a[52:53], v25 offset:256\n"
      "ds_read_b64_tr_b16 a[54:55], v25 offset:1280\n"
      "buffer_load_dwordx4 v[68:71], v32, s[36:39], s66 offen offset:1024 sc1\n"
      "ds_read_b64_tr_b16 a[56:57], v25 offset:2048\n"
      "ds_read_b64_tr_b16 a[58:59], v25 offset:3072\n"
      "ds_read_b64_tr_b16 a[60:61], v25 offset:2304\n"
      "ds_read_b64_tr_b16 a[62:63], v25 offset:3328\n"
      "s_mov_b32 s66, 16384\n"
      "buffer_load_dwordx4 v[72:75], v32, s[36:39], s66 offen offset:0 sc1\n"
      "ds_read_b64_tr_b16 a[64:65], v25 offset:4096\n"
      "ds_read_b64_tr_b16 a[66:67], v25 offset:5120\n"
      "ds_read_b64_tr_b16 a[68:69], v25 offset:4352\n"
      "ds_read_b64_tr_b16 a[70:71], v25 offset:5376\n"
      "buffer_load_dwordx4 v[76:79], v32, s[36:39], s66 offen offset:1024 sc1\n"
      "ds_read_b64_tr_b16 a[72:73], v25 offset:6144\n"
      "ds_read_b64_tr_b16 a[74:75], v25 offset:7168\n"
      "ds_read_b64_tr_b16 a[76:77], v25 offset:6400\n"
      "ds_read_b64_tr_b16 a[78:79], v25 offset:7424\n"
      "s_mov_b32 s66, 24576\n"
      "buffer_load_dwordx4 v[80:83], v32, s[36:39], s66 offen offset:0 sc1\n"
      "buffer_load_dwordx4 v[84:87], v32, s[36:39], s66 offen offset:1024 sc1\n"
      "s_mov_b32 s66, 32768\n"
      "buffer_load_dwordx4 v[88:91], v32, s[36:39], s66 offen offset:0 sc1\n"
      "s_waitcnt vmcnt(9)\n"
      "s_barrier\n"
      "s_mov_b64 exec, s[8:9]\n"
      "buffer_atomic_swap v8, v7, s[36:39], 0 offen offset:64\n"
      "s_mov_b64 exec, -1\n"
      "buffer_load_dwordx4 v[92:95], v32, s[36:39], s66 offen offset:1024 sc1\n"
      "s_mov_b32 s66, 40960\n"
      "buffer_load_dwordx4 v[96:99], v32, s[36:39], s66 offen offset:0 sc1\n"
      "s_waitcnt vmcnt(11)\n"
      "v_mfma_f32_32x32x16_bf16 v[208:223], a[116:119], v[56:59], 0\n"
      "buffer_load_dwordx4 v[100:103], v32, s[36:39], s66 offen offset:1024 sc1\n"
      "v_mfma_f32_32x32x16_bf16 v[192:207], a[112:115], v[56:59], 0\n"
      "v_lshlrev_b32 v176, 16, v128\n"
      "v_and_b32 v177, 0xffff0000, v128\n"
      "v_lshlrev_b32 v2, 16, v34\n"
      "v_add_f32 v176, v176, v2\n"
      "v_and_b32 v2, 0xffff0000, v34\n"
      "s_waitcnt vmcnt(11)\n"
      "v_mfma_f32_32x32x16_bf16 v[208:223], a[124:127], v[60:63], v[208:223]\n"
      "s_mov_b32 s66, 49152\n"
      "buffer_load_dwordx4 v[104:107], v32, s[36:39], s66 offen offset:0 sc1\n"
      "v_mfma_f32_32x32x16_bf16 v[192:207], a[120:123], v[60:63], v[192:207]\n"
      "v_add_f32 v177, v177, v2\n"
      "v_lshlrev_b32 v178, 16, v129\n"
      "v_and_b32 v179, 0xffff0000, v129\n"
      "v_lshlrev_b32 v2, 16, v35\n"
      "v_add_f32 v178, v178, v2\n"
      "s_waitcnt vmcnt(11)\n"
      "v_mfma_f32_32x32x16_bf16 v[208:223], a[132:135], v[64:67], v[208:223]\n"
      "buffer_load_dwordx4 v[108:111], v32, s[36:39], s66 offen offset:1024 sc1\n"
      "v_mfma_f32_32x32x16_bf16 v[192:207], a[128:131], v[64:67], v[192:207]\n"
      "v_and_b32 v2, 0xffff0000, v35\n"
      "v_add_f32 v179, v179, v2\n"
      "v_lshlrev_b32 v180, 16, v130\n"
      "v_and_b32 v181, 0xffff0000, v130\n"
      "v_lshlrev_b32 v2, 16, v36\n"
      "s_waitcnt vmcnt(11)\n"
      "v_mfma_f32_32x32x16_bf16 v[208:223], a[140:143], v[68:71], v[208:223]\n"
      "s_mov_b32 s66, 57344\n"
      "buffer_load_dwordx4 v[112:115], v32, s[36:39], s66 offen offset:0 sc1\n"
      "v_mfma_f32_32x32x16_bf16 v[192:207], a[136:139], v[68:71], v[192:207]\n"
      "v_mov_b32 v3, v8\n"
      "s_mov_b64 exec, 0x3fff\n"
      "v_mov_b32 v3, 0\n"
      "buffer_atomic_or v3, v4, s[36:39], 0 offen offset:64 sc0\n"
      "s_mov_b64 exec, -1\n"
      "s_waitcnt vmcnt(12)\n"
      "v_mfma_f32_32x32x16_bf16 v[208:223], a[148:151], v[72:75], v[208:223]\n"
      "buffer_load_dwordx4 v[116:119], v32, s[36:39], s66 offen offset:1024 sc1\n"
      "v_mfma_f32_32x32x16_bf16 v[192:207], a[144:147], v[72:75], v[192:207]\n"
      "v_add_f32 v180, v180, v2\n"
      "v_and_b32 v2, 0xffff0000, v36\n"
      "v_add_f32 v181, v181, v2\n"
      "v_lshlrev_b32 v182, 16, v131\n"
      "v_and_b32 v183, 0xffff0000, v131\n"
      "s_waitcnt vmcnt(12)\n"
      "v_mfma_f32_32x32x16_bf16 v[208:223], a[156:159], v[76:79], v[208:223]\n"
      "s_mov_b32 s66, 65536\n"
      "buffer_load_dwordx4 v[120:123], v32, s[36:39], s66 offen offset:0 sc1\n"
      "v_mfma_f32_32x32x16_bf16 v[192:207], a[152:155], v[76:79], v[192:207]\n"
      "v_lshlrev_b32 v2, 16, v37\n"
      "v_add_f32 v182, v182, v2\n"
      "v_and_b32 v2, 0xffff0000, v37\n"
      "v_add_f32 v183, v183, v2\n"
      "v_lshlrev_b32 v184, 16, v132\n"
      "s_waitcnt vmcnt(12)\n"
      "v_mfma_f32_32x32x16_bf16 v[208:223], a[164:167], v[80:83], v[208:223]\n"
      "buffer_load_dwordx4 v[124:127], v32, s[36:39], s66 offen offset:1024 sc1\n"
      "v_mfma_f32_32x32x16_bf16 v[192:207], a[160:163], v[80:83], v[192:207]\n"
      "v_and_b32 v185, 0xffff0000, v132\n"
      "v_lshlrev_b32 v2, 16, v34\n"
      "v_add_f32 v184, v184, v2\n"
      "v_and_b32 v2, 0xffff0000, v34\n"
      "v_add_f32 v185, v185, v2\n"
      "s_waitcnt vmcnt(12)\n"
      "v_mfma_f32_32x32x16_bf16 v[208:223], a[172:175], v[84:87], v[208:223]\n"
      "s_mov_b32 s66, 73728\n"
      "buffer_load_dwordx4 v[56:59], v32, s[36:39], s66 offen offset:0 sc1\n"
      "v_mfma_f32_32x32x16_bf16 v[192:207], a[168:171], v[84:87], v[192:207]\n"
      "v_lshlrev_b32 v186, 16, v133\n"
      "v_and_b32 v187, 0xffff0000, v133\n"
      "v_lshlrev_b32 v2, 16, v35\n"
      "v_add_f32 v186, v186, v2\n"
      "v_and_b32 v2, 0xffff0000, v35\n"
      "s_waitcnt vmcnt(12)\n"
      "v_mfma_f32_32x32x16_bf16 v[208:223], a[180:183], v[88:91], v[208:223]\n"
      "buffer_load_dwordx4 v[60:63], v32, s[36:39], s66 offen offset:1024 sc1\n"
      "v_mfma_f32_32x32x16_bf16 v[192:207], a[176:179], v[88:91], v[192:207]\n"
      "v_add_f32 v187, v187, v2\n"
      "v_lshlrev_b32 v188, 16, v134\n"
      "v_and_b32 v189, 0xffff0000, v134\n"
      "v_lshlrev_b32 v2, 16, v36\n"
      "v_add_f32 v188, v188, v2\n"
      "s_waitcnt vmcnt(11)\n"
      "v_mfma_f32_32x32x16_bf16 v[208:223], a[188:191], v[92:95], v[208:223]\n"
      "s_mov_b32 s66, 81920\n"
      "buffer_load_dwordx4 v[64:67], v32, s[36:39], s66 offen offset:0 sc1\n"
      "v_mfma_f32_32x32x16_bf16 v[192:207], a[184:187], v[92:95], v[192:207]\n"
      "v_and_b32 v2, 0xffff0000, v36\n"
      "v_add_f32 v189, v189, v2\n"
      "v_lshlrev_b32 v190, 16, v135\n"
      "v_and_b32 v191, 0xffff0000, v135\n"
      "v_lshlrev_b32 v2, 16, v37\n"
      "s_waitcnt vmcnt(11)\n"
      "v_mfma_f32_32x32x16_bf16 v[208:223], a[196:199], v[96:99], v[208:223]\n"
      "buffer_load_dwordx4 v[68:71], v32, s[36:39], s66 offen offset:1024 sc1\n"
      "v_mfma_f32_32x32x16_bf16 v[192:207], a[192:195], v[96:99], v[192:207]\n"
      "v_add_f32 v190, v190, v2\n"
      "v_and_b32 v2, 0xffff0000, v37\n"
      "v_add_f32 v191, v191, v2\n"
      "s_waitcnt vmcnt(11)\n"
      "v_mfma_f32_32x32x16_bf16 v[208:223], a[204:207], v[100:103], v[208:223]\n"
      "s_mov_b32 s66, 90112\n"
      "buffer_load_dwordx4 v[72:75], v32, s[36:39], s66 offen offset:0 sc1\n"
      "v_mfma_f32_32x32x16_bf16 v[192:207], a[200:203], v[100:103], v[192:207]\n"
      "s_waitcnt vmcnt(11)\n"
      "v_mfma_f32_32x32x16_bf16 v[208:223], a[212:215], v[104:107], v[208:223]\n"
      "buffer_load_dwordx4 v[76:79], v32, s[36:39], s66 offen offset:1024 sc1\n"
      "v_mfma_f32_32x32x16_bf16 v[192:207], a[208:211], v[104:107], v[192:207]\n"
      "s_waitcnt vmcnt(11)\n"
      "v_mfma_f32_32x32x16_bf16 v[208:223], a[220:223], v[108:111], v[208:223]\n"
      "s_mov_b32 s66, 98304\n"
      "buffer_load_dwordx4 v[80:83], v32, s[36:39], s66 offen offset:0 sc1\n"
      "v_mfma_f32_32x32x16_bf16 v[192:207], a[216:219], v[108:111], v[192:207]\n"
      "s_waitcnt vmcnt(11)\n"
      "v_mfma_f32_32x32x16_bf16 v[208:223], a[228:231], v[112:115], v[208:223]\n"
      "buffer_load_dwordx4 v[84:87], v32, s[36:39], s66 offen offset:1024 sc1\n"
      "v_mfma_f32_32x32x16_bf16 v[192:207], a[224:227], v[112:115], v[192:207]\n"
      "s_waitcnt vmcnt(10)\n"
      "v_mfma_f32_32x32x16_bf16 v[208:223], a[236:239], v[116:119], v[208:223]\n"
      "s_mov_b32 s66, 106496\n"
      "buffer_load_dwordx4 v[88:91], v32, s[36:39], s66 offen offset:0 sc1\n"
      "v_mfma_f32_32x32x16_bf16 v[192:207], a[232:235], v[116:119], v[192:207]\n"
      "v_cmp_ne_u32 vcc, s73, v3\n"
      "s_nop 0\n"
      "s_cbranch_vccnz .Lk12_p4lslow_%=\n"
      ".Lk13_p4lback_%=:\n"
      "s_waitcnt vmcnt(10)\n"
      "v_mfma_f32_32x32x16_bf16 v[208:223], a[244:247], v[120:123], v[208:223]\n"
      "buffer_load_dwordx4 v[92:95], v32, s[36:39], s66 offen offset:1024 sc1\n"
      "v_mfma_f32_32x32x16_bf16 v[192:207], a[240:243], v[120:123], v[192:207]\n"
      "s_waitcnt vmcnt(10) lgkmcnt(14)\n"
      "v_mfma_f32_32x32x16_bf16 v[208:223], a[252:255], v[124:127], v[208:223]\n"
      "buffer_load_dwordx4 v[96:99], v33, s[36:39], 0 offen offset:0 sc1\n"
      "v_mfma_f32_32x32x16_bf16 v[192:207], a[248:251], v[124:127], v[192:207]\n"
      "s_waitcnt vmcnt(10)\n"
      "v_mfma_f32_32x32x16_bf16 v[208:223], a[4:7], v[56:59], v[208:223]\n"
      "buffer_load_dwordx4 v[100:103], v33, s[36:39], 0 offen offset:1024 sc1\n"
      "v_mfma_f32_32x32x16_bf16 v[192:207], a[0:3], v[56:59], v[192:207]\n"
      "s_waitcnt vmcnt(10)\n"
      "v_mfma_f32_32x32x16_bf16 v[208:223], a[12:15], v[60:63], v[208:223]\n"
      "s_mov_b32 s66, 8192\n"
      "buffer_load_dwordx4 v[104:107], v33, s[36:39], s66 offen offset:0 sc1\n"
      "v_mfma_f32_32x32x16_bf16 v[192:207], a[8:11], v[60:63], v[192:207]\n"
      "s_waitcnt vmcnt(10)\n"
      "v_mfma_f32_32x32x16_bf16 v[208:223], a[20:23], v[64:67], v[208:223]\n"
      "buffer_load_dwordx4 v[108:111], v33, s[36:39], s66 offen offset:1024 sc1\n"
      "v_mfma_f32_32x32x16_bf16 v[192:207], a[16:19], v[64:67], v[192:207]\n"
      "s_waitcnt vmcnt(10)\n"
      "v_mfma_f32_32x32x16_bf16 v[208:223], a[28:31], v[68:71], v[208:223]\n"
      "s_mov_b32 s66, 16384\n"
      "buffer_load_dwordx4 v[112:115], v33, s[36:39], s66 offen offset:0 sc1\n"
      "v_mfma_f32_32x32x16_bf16 v[192:207], a[24:27], v[68:71], v[192:207]\n"
      "s_waitcnt vmcnt(10)\n"
      "v_mfma_f32_32x32x16_bf16 v[208:223], a[36:39], v[72:75], v[208:223]\n"
      "buffer_load_dwordx4 v[116:119], v33, s[36:39], s66 offen offset:1024 sc1\n"
      "v_mfma_f32_32x32x16_bf16 v[192:207], a[32:35], v[72:75], v[192:207]\n"
      "s_waitcnt vmcnt(10)\n"
      "v_mfma_f32_32x32x16_bf16 v[208:223], a[44:47], v[76:79], v[208:223]\n"
      "s_mov_b32 s66, 24576\n"
      "buffer_load_dwordx4 v[120:123], v33, s[36:39], s66 offen offset:0 sc1\n"
      "v_mfma_f32_32x32x16_bf16 v[192:207], a[40:43], v[76:79], v[192:207]\n"
      "s_waitcnt vmcnt(10) lgkmcnt(12)\n"
      "v_mfma_f32_32x32x16_bf16 v[208:223], a[52:55], v[80:83], v[208:223]\n"
      "buffer_load_dwordx4 v[124:127], v33, s[36:39], s66 offen offset:1024 sc1\n"
      "v_mfma_f32_32x32x16_bf16 v[192:207], a[48:51], v[80:83], v[192:207]\n"
      "s_waitcnt vmcnt(10) lgkmcnt(8)\n"
      "v_mfma_f32_32x32x16_bf16 v[208:223], a[60:63], v[84:87], v[208:223]\n"
      "s_mov_b32 s66, 32768\n"
      "buffer_load_dwordx4 v[56:59], v33, s[36:39], s66 offen offset:0 sc1\n"
      "v_mfma_f32_32x32x16_bf16 v[192:207], a[56:59], v[84:87], v[192:207]\n"
      "s_waitcnt vmcnt(10) lgkmcnt(4)\n"
      "v_mfma_f32_32x32x16_bf16 v[208:223], a[68:71], v[88:91], v[208:223]\n"
      "buffer_load_dwordx4 v[60:63], v33, s[36:39], s66 offen offset:1024 sc1\n"
      "v_mfma_f32_32x32x16_bf16 v[192:207], a[64:67], v[88:91], v[192:207]\n"
      "s_waitcnt vmcnt(10) lgkmcnt(0)\n"
      "v_mfma_f32_32x32x16_bf16 v[208:223], a[76:79], v[92:95], v[208:223]\n"
      "s_mov_b32 s66, 40960\n"
      "buffer_load_dwordx4 v[64:67], v33, s[36:39], s66 offen offset:0 sc1\n"
      "v_mfma_f32_32x32x16_bf16 v[192:207], a[72:75], v[92:95], v[192:207]\n"
      "s_waitcnt vmcnt(10)\n"
      "v_mfma_f32_32x32x16_bf16 v[240:255], a[116:119], v[96:99], 0\n"
      "buffer_load_dwordx4 v[68:71], v33, s[36:39], s66 offen offset:1024 sc1\n"
      "v_mfma_f32_32x32x16_bf16 v[224:239], a[112:115], v[96:99], 0\n"
      "s_waitcnt vmcnt(10)\n"
      "v_mfma_f32_32x32x16_bf16 v[240:255], a[124:127], v[100:103], v[240:255]\n"
      "s_mov_b32 s66, 49152\n"
      "buffer_load_dwordx4 v[72:75], v33, s[36:39], s66 offen offset:0 sc1\n"
      "v_mfma_f32_32x32x16_bf16 v[224:239], a[120:123], v[100:103], v[224:239]\n"
      "s_waitcnt vmcnt(10)\n"
      "v_mfma_f32_32x32x16_bf16 v[240:255], a[132:135], v[104:107], v[240:255]\n"
      "buffer_load_dwordx4 v[76:79], v33, s[36:39], s66 offen offset:1024 sc1\n"
      "v_mfma_f32_32x32x16_bf16 v[224:239], a[128:131], v[104:107], v[224:239]\n"
      "ds_write_b128 v39, v[216:219] offset:4096\n"
      "s_waitcnt vmcnt(10)\n"
      "v_mfma_f32_32x32x16_bf16 v[240:255], a[140:143], v[108:111], v[240:255]\n"
      "s_mov_b32 s66, 57344\n"
      "buffer_load_dwordx4 v[80:83], v33, s[36:39], s66 offen offset:0 sc1\n"
      "v_mfma_f32_32x32x16_bf16 v[224:239], a[136:139], v[108:111], v[224:239]\n"
      "ds_write_b128 v39, v[220:223] offset:5120\n"
      "s_waitcnt vmcnt(10)\n"
      "v_mfma_f32_32x32x16_bf16 v[240:255], a[148:151], v[112:115], v[240:255]\n"
      "buffer_load_dwordx4 v[84:87], v33, s[36:39], s66 offen offset:1024 sc1\n"
      "v_mfma_f32_32x32x16_bf16 v[224:239], a[144:147], v[112:115], v[224:239]\n"
      "ds_write_b128 v39, v[208:211] offset:2048\n"
      "s_waitcnt vmcnt(10)\n"
      "v_mfma_f32_32x32x16_bf16 v[240:255], a[156:159], v[116:119], v[240:255]\n"
      "s_mov_b32 s66, 65536\n"
      "buffer_load_dwordx4 v[88:91], v33, s[36:39], s66 offen offset:0 sc1\n"
      "v_mfma_f32_32x32x16_bf16 v[224:239], a[152:155], v[116:119], v[224:239]\n"
      "ds_write_b128 v39, v[212:215] offset:3072\n"
      "s_waitcnt vmcnt(10)\n"
      "v_mfma_f32_32x32x16_bf16 v[240:255], a[164:167], v[120:123], v[240:255]\n"
      "buffer_load_dwordx4 v[92:95], v33, s[36:39], s66 offen offset:1024 sc1\n"
      "v_mfma_f32_32x32x16_bf16 v[224:239], a[160:163], v[120:123], v[224:239]\n"
      "ds_write_b128 v39, v[200:203] offset:0\n"
      "s_waitcnt vmcnt(10)\n"
      "v_mfma_f32_32x32x16_bf16 v[240:255], a[172:175], v[124:127], v[240:255]\n"
      "s_mov_b32 s66, 73728\n"
      "buffer_load_dwordx4 v[96:99], v33, s[36:39], s66 offen offset:0 sc1\n"
      "v_mfma_f32_32x32x16_bf16 v[224:239], a[168:171], v[124:127], v[224:239]\n"
      "ds_write_b128 v39, v[204:207] offset:1024\n"
      "s_waitcnt vmcnt(10)\n"
      "v_mfma_f32_32x32x16_bf16 v[240:255], a[180:183], v[56:59], v[240:255]\n"
      "buffer_load_dwordx4 v[100:103], v33, s[36:39], s66 offen offset:1024 sc1\n"
      "v_mfma_f32_32x32x16_bf16 v[224:239], a[176:179], v[56:59], v[224:239]\n"
      "s_waitcnt vmcnt(10)\n"
      "v_mfma_f32_32x32x16_bf16 v[240:255], a[188:191], v[60:63], v[240:255]\n"
      "s_mov_b32 s66, 81920\n"
      "buffer_load_dwordx4 v[104:107], v33, s[36:39], s66 offen offset:0 sc1\n"
      "v_mfma_f32_32x32x16_bf16 v[224:239], a[184:187], v[60:63], v[224:239]\n"
      "s_waitcnt vmcnt(10)\n"
      "v_mfma_f32_32x32x16_bf16 v[240:255], a[196:199], v[64:67], v[240:255]\n"
      "buffer_load_dwordx4 v[108:111], v33, s[36:39], s66 offen offset:1024 sc1\n"
      "v_mfma_f32_32x32x16_bf16 v[224:239], a[192:195], v[64:67], v[224:239]\n"
      "s_waitcnt lgkmcnt(0)\n"
      "s_barrier\n"
      "s_waitcnt vmcnt(10)\n"
      "v_mfma_f32_32x32x16_bf16 v[240:255], a[204:207], v[68:71], v[240:255]\n"
      "s_mov_b32 s66, 90112\n"
      "buffer_load_dwordx4 v[112:115], v33, s[36:39], s66 offen offset:0 sc1\n"
      "ds_read_b128 v[152:155], v40 offset:0\n"
      "ds_read_b128 v[160:163], v41 offset:2048\n"
      "ds_read_b128 v[168:171], v42 offset:4096\n"
      "v_mfma_f32_32x32x16_bf16 v[224:239], a[200:203], v[68:71], v[224:239]\n"
      "ds_read_b128 v[156:159], v40 offset:1024\n"
      "ds_read_b128 v[164:167], v41 offset:3072\n"
      "ds_read_b128 v[172:175], v42 offset:5120\n"
      "s_waitcnt vmcnt(10)\n"
      "v_mfma_f32_32x32x16_bf16 v[240:255], a[212:215], v[72:75], v[240:255]\n"
      "buffer_load_dwordx4 v[116:119], v33, s[36:39], s66 offen offset:1024 sc1\n"
      "v_mfma_f32_32x32x16_bf16 v[224:239], a[208:211], v[72:75], v[224:239]\n"
      "s_waitcnt vmcnt(10)\n"
      "v_mfma_f32_32x32x16_bf16 v[240:255], a[220:223], v[76:79], v[240:255]\n"
      "s_mov_b32 s66, 98304\n"
      "buffer_load_dwordx4 v[120:123], v33, s[36:39], s66 offen offset:0 sc1\n"
      "s_waitcnt lgkmcnt(5)\n"
      "v_add_f32 v192, v192, v152\n"
      "s_waitcnt lgkmcnt(3)\n"
      "v_add_f32 v160, v160, v168\n"
      "v_add_f32 v192, v192, v160\n"
      "v_mfma_f32_32x32x16_bf16 v[224:239], a[216:219], v[76:79], v[224:239]\n"
      "v_fma_f32 v192, v192, v30, v176\n"
      "v_add_f32 v193, v193, v153\n"
      "v_add_f32 v161, v161, v169\n"
      "v_add_f32 v193, v193, v161\n"
      "s_waitcnt vmcnt(10)\n"
      "v_mfma_f32_32x32x16_bf16 v[240:255], a[228:231], v[80:83], v[240:255]\n"
      "buffer_load_dwordx4 v[124:127], v33, s[36:39], s66 offen offset:1024 sc1\n"
      "v_fma_f32 v193, v193, v30, v177\n"
      "v_cvt_pk_bf16_f32 v18, v192, v193\n"
      "v_add_f32 v194, v194, v154\n"
      "v_mfma_f32_32x32x16_bf16 v[224:239], a[224:227], v[80:83], v[224:239]\n"
      "v_add_f32 v162, v162, v170\n"
      "v_add_f32 v194, v194, v162\n"
      "v_fma_f32 v194, v194, v30, v178\n"
      "v_add_f32 v195, v195, v155\n"
      "s_waitcnt vmcnt(10)\n"
      "v_mfma_f32_32x32x16_bf16 v[240:255], a[236:239], v[84:87], v[240:255]\n"
      "s_mov_b32 s66, 106496\n"
      "buffer_load_dwordx4 v[56:59], v33, s[36:39], s66 offen offset:0 sc1\n"
      "v_add_f32 v163, v163, v171\n"
      "v_add_f32 v195, v195, v163\n"
      "v_fma_f32 v195, v195, v30, v179\n"
      "v_mfma_f32_32x32x16_bf16 v[224:239], a[232:235], v[84:87], v[224:239]\n"
      "v_cvt_pk_bf16_f32 v19, v194, v195\n"
      "s_waitcnt lgkmcnt(2)\n"
      "v_add_f32 v196, v196, v156\n"
      "s_waitcnt lgkmcnt(0)\n"
      "v_add_f32 v164, v164, v172\n"
      "v_add_f32 v196, v196, v164\n"
      "s_waitcnt vmcnt(10)\n"
      "v_mfma_f32_32x32x16_bf16 v[240:255], a[244:247], v[88:91], v[240:255]\n"
      "buffer_load_dwordx4 v[60:63], v33, s[36:39], s66 offen offset:1024 sc1\n"
      "v_fma_f32 v196, v196, v30, v180\n"
      "v_add_f32 v197, v197, v157\n"
      "v_add_f32 v165, v165, v173\n"
      "v_mfma_f32_32x32x16_bf16 v[224:239], a[240:243], v[88:91], v[224:239]\n"
      "v_add_f32 v197, v197, v165\n"
      "v_fma_f32 v197, v197, v30, v181\n"
      "v_cvt_pk_bf16_f32 v20, v196, v197\n"
      "v_add_f32 v198, v198, v158\n"
      "s_waitcnt vmcnt(10)\n"
      "v_mfma_f32_32x32x16_bf16 v[240:255], a[252:255], v[92:95], v[240:255]\n"
      "v_add_f32 v166, v166, v174\n"
      "v_add_f32 v198, v198, v166\n"
      "v_fma_f32 v198, v198, v30, v182\n"
      "v_mfma_f32_32x32x16_bf16 v[224:239], a[248:251], v[92:95], v[224:239]\n"
      "v_add_f32 v199, v199, v159\n"
      "v_add_f32 v167, v167, v175\n"
      "v_add_f32 v199, v199, v167\n"
      "v_fma_f32 v199, v199, v30, v183\n"
      "s_waitcnt vmcnt(9)\n"
      "v_mfma_f32_32x32x16_bf16 v[240:255], a[4:7], v[96:99], v[240:255]\n"
      "v_cvt_pk_bf16_f32 v21, v198, v199\n"
      "v_permlane32_swap_b32 v18, v20\n"
      "s_nop 0\n"
      "v_permlane32_swap_b32 v19, v21\n"
      "v_mfma_f32_32x32x16_bf16 v[224:239], a[0:3], v[96:99], v[224:239]\n"
      "buffer_store_dwordx4 v[18:21], v27, s[28:31], 0 offen\n"
      "s_waitcnt vmcnt(9)\n"
      "v_mfma_f32_32x32x16_bf16 v[240:255], a[12:15], v[100:103], v[240:255]\n"
      "v_mfma_f32_32x32x16_bf16 v[224:239], a[8:11], v[100:103], v[224:239]\n"
      "s_waitcnt vmcnt(8)\n"
      "v_mfma_f32_32x32x16_bf16 v[240:255], a[20:23], v[104:107], v[240:255]\n"
      "v_mfma_f32_32x32x16_bf16 v[224:239], a[16:19], v[104:107], v[224:239]\n"
      "s_waitcnt vmcnt(7)\n"
      "v_mfma_f32_32x32x16_bf16 v[240:255], a[28:31], v[108:111], v[240:255]\n"
      "v_mfma_f32_32x32x16_bf16 v[224:239], a[24:27], v[108:111], v[224:239]\n"
      "s_waitcnt vmcnt(6)\n"
      "v_mfma_f32_32x32x16_bf16 v[240:255], a[36:39], v[112:115], v[240:255]\n"
      "v_mfma_f32_32x32x16_bf16 v[224:239], a[32:35], v[112:115], v[224:239]\n"
      "s_waitcnt vmcnt(5)\n"
      "v_mfma_f32_32x32x16_bf16 v[240:255], a[44:47], v[116:119], v[240:255]\n"
      "v_mfma_f32_32x32x16_bf16 v[224:239], a[40:43], v[116:119], v[224:239]\n"
      "s_waitcnt vmcnt(4)\n"
      "v_mfma_f32_32x32x16_bf16 v[240:255], a[52:55], v[120:123], v[240:255]\n"
      "v_mfma_f32_32x32x16_bf16 v[224:239], a[48:51], v[120:123], v[224:239]\n"
      "s_waitcnt vmcnt(3)\n"
      "v_mfma_f32_32x32x16_bf16 v[240:255], a[60:63], v[124:127], v[240:255]\n"
      "v_mfma_f32_32x32x16_bf16 v[224:239], a[56:59], v[124:127], v[224:239]\n"
      "s_waitcnt vmcnt(2)\n"
      "v_mfma_f32_32x32x16_bf16 v[240:255], a[68:71], v[56:59], v[240:255]\n"
      "v_mfma_f32_32x32x16_bf16 v[224:239], a[64:67], v[56:59], v[224:239]\n"
      "s_waitcnt vmcnt(1)\n"
      "v_mfma_f32_32x32x16_bf16 v[240:255], a[76:79], v[60:63], v[240:255]\n"
      "v_mfma_f32_32x32x16_bf16 v[224:239], a[72:75], v[60:63], v[224:239]\n"
      "s_nop 10\n"
      "ds_write_b128 v39, v[248:251] offset:10240\n"
      "ds_write_b128 v39, v[252:255] offset:11264\n"
      "ds_write_b128 v39, v[240:243] offset:8192\n"
      "ds_write_b128 v39, v[244:247] offset:9216\n"
      "ds_write_b128 v39, v[232:235] offset:6144\n"
      "ds_write_b128 v39, v[236:239] offset:7168\n"
      "s_waitcnt lgkmcnt(0)\n"
      "s_barrier\n"
      "ds_read_b128 v[152:155], v40 offset:6144\n"
      "ds_read_b128 v[160:163], v41 offset:8192\n"
      "ds_read_b128 v[168:171], v42 offset:10240\n"
      "ds_read_b128 v[156:159], v40 offset:7168\n"
      "ds_read_b128 v[164:167], v41 offset:9216\n"
      "ds_read_b128 v[172:175], v42 offset:11264\n"
      "s_waitcnt lgkmcnt(5)\n"
      "v_pk_add_f32 v[224:225], v[224:225], v[152:153]\n"
      "s_waitcnt lgkmcnt(3)\n"
      "v_pk_add_f32 v[160:161], v[160:161], v[168:169]\n"
      "v_pk_add_f32 v[224:225], v[224:225], v[160:161]\n"
      "v_pk_fma_f32 v[224:225], v[224:225], v[30:31], v[184:185] op_sel:[0,1,0] op_sel_hi:[1,1,1]\n"
      "v_cvt_pk_bf16_f32 v18, v224, v225\n"
      "v_pk_add_f32 v[226:227], v[226:227], v[154:155]\n"
      "v_pk_add_f32 v[162:163], v[162:163], v[170:171]\n"
      "v_pk_add_f32 v[226:227], v[226:227], v[162:163]\n"
      "v_pk_fma_f32 v[226:227], v[226:227], v[30:31], v[186:187] op_sel:[0,1,0] op_sel_hi:[1,1,1]\n"
      "v_cvt_pk_bf16_f32 v19, v226, v227\n"
      "s_waitcnt lgkmcnt(2)\n"
      "v_pk_add_f32 v[228:229], v[228:229], v[156:157]\n"
      "s_waitcnt lgkmcnt(0)\n"
      "v_pk_add_f32 v[164:165], v[164:165], v[172:173]\n"
      "v_pk_add_f32 v[228:229], v[228:229], v[164:165]\n"
      "v_pk_fma_f32 v[228:229], v[228:229], v[30:31], v[188:189] op_sel:[0,1,0] op_sel_hi:[1,1,1]\n"
      "v_cvt_pk_bf16_f32 v20, v228, v229\n"
      "v_pk_add_f32 v[230:231], v[230:231], v[158:159]\n"
      "v_pk_add_f32 v[166:167], v[166:167], v[174:175]\n"
      "v_pk_add_f32 v[230:231], v[230:231], v[166:167]\n"
      "v_pk_fma_f32 v[230:231], v[230:231], v[30:31], v[190:191] op_sel:[0,1,0] op_sel_hi:[1,1,1]\n"
      "v_cvt_pk_bf16_f32 v21, v230, v231\n"
      "v_permlane32_swap_b32 v18, v20\n"
      "s_nop 0\n"
      "v_permlane32_swap_b32 v19, v21\n"
      "buffer_store_dwordx4 v[18:21], v27, s[28:31], s11 offen\n"
      "s_branch .Lk5_end_%=\n"
      ".Lk6_p2slow_%=:\n"
      "s_mov_b32 s10, 200000\n"
      "s_mov_b64 exec, 0xffff\n"
      ".Lk14_p2l_%=:\n"
      "v_mov_b32 v3, 0\n"
      "buffer_atomic_or v3, v4, s[36:39], 0 offen offset:0 sc0\n"
      "s_waitcnt vmcnt(0)\n"
      "v_cmp_ne_u32 vcc, s73, v3\n"
      "s_nop 0\n"
      "s_cbranch_vccz .Lk15_p2lok_%=\n"
      "s_sub_u32 s10, s10, 1\n"
      "s_cbranch_scc0 .Lk14_p2l_%=\n"
      ".Lk15_p2lok_%=:\n"
      "s_mov_b64 exec, -1\n"
      "s_branch .Lk7_p2back_%=\n"
      ".Lk8_p3slow_%=:\n"
      "s_mov_b32 s10, 200000\n"
      "s_mov_b64 exec, 0x3fff\n"
      ".Lk16_p3l_%=:\n"
      "v_mov_b32 v3, 0\n"
      "buffer_atomic_or v3, v4, s[36:39], 0 offen offset:0 sc0\n"
      "s_waitcnt vmcnt(0)\n"
      "v_cmp_ne_u32 vcc, s73, v3\n"
      "s_nop 0\n"
      "s_cbranch_vccz .Lk17_p3lok_%=\n"
      "s_sub_u32 s10, s10, 1\n"
      "s_cbranch_scc0 .Lk16_p3l_%=\n"
      ".Lk17_p3lok_%=:\n"
      "s_mov_b64 exec, -1\n"
      "s_branch .Lk9_p3back_%=\n"
      ".Lk10_p4slow_%=:\n"
      "s_mov_b32 s10, 200000\n"
      "s_mov_b64 exec, 0x3fff\n"
      ".Lk18_p4l_%=:\n"
      "v_mov_b32 v3, 0\n"
      "buffer_atomic_or v3, v4, s[36:39], 0 offen offset:0 sc0\n"
      "s_waitcnt vmcnt(0)\n"
      "v_cmp_ne_u32 vcc, s73, v3\n"
      "s_nop 0\n"
      "s_cbranch_vccz .Lk19_p4lok_%=\n"
      "s_sub_u32 s10, s10, 1\n"
      "s_cbranch_scc0 .Lk18_p4l_%=\n"
      ".Lk19_p4lok_%=:\n"
      "s_mov_b64 exec, -1\n"
      "s_branch .Lk11_p4back_%=\n"
      ".Lk12_p4lslow_%=:\n"
      "s_mov_b32 s10, 200000\n"
      "s_mov_b64 exec, 0x3fff\n"
      ".Lk20_p4ll_%=:\n"
      "v_mov_b32 v3, 0\n"
      "buffer_atomic_or v3, v4, s[36:39], 0 offen offset:64 sc0\n"
      "s_waitcnt vmcnt(0)\n"
      "v_cmp_ne_u32 vcc, s73, v3\n"
      "s_nop 0\n"
      "s_cbranch_vccz .Lk21_p4llok_%=\n"
      "s_sub_u32 s10, s10, 1\n"
      "s_cbranch_scc0 .Lk20_p4ll_%=\n"
      ".Lk21_p4llok_%=:\n"
      "s_mov_b64 exec, -1\n"
      "s_branch .Lk13_p4lback_%=\n"
      ".Lk3_kvslow_%=:\n"
      "s_mov_b32 s46, 0\n"
      "v_bitop3_b32 v2, v64, v66, s73 bitop3:0x7e\n"
      "v_cmp_ne_u32 vcc, 0, v2\n"
      "s_nop 0\n"
      "s_cbranch_vccz .Lk22_kvskip_%=\n"
      "s_mov_b32 s66, 0\n"
      "buffer_load_dwordx4 v[64:67], v5, s[36:39], s66 offen sc1\n"
      "s_mov_b32 s46, 1\n"
      ".Lk22_kvskip_%=:\n"
      "v_bitop3_b32 v2, v68, v70, s73 bitop3:0x7e\n"
      "v_cmp_ne_u32 vcc, 0, v2\n"
      "s_nop 0\n"
      "s_cbranch_vccz .Lk23_kvskip_%=\n"
      "s_mov_b32 s66, 4096\n"
      "buffer_load_dwordx4 v[68:71], v5, s[36:39], s66 offen sc1\n"
      "s_mov_b32 s46, 1\n"
      ".Lk23_kvskip_%=:\n"
      "v_bitop3_b32 v2, v72, v74, s73 bitop3:0x7e\n"
      "v_cmp_ne_u32 vcc, 0, v2\n"
      "s_nop 0\n"
      "s_cbranch_vccz .Lk24_kvskip_%=\n"
      "s_mov_b32 s66, 8192\n"
      "buffer_load_dwordx4 v[72:75], v5, s[36:39], s66 offen sc1\n"
      "s_mov_b32 s46, 1\n"
      ".Lk24_kvskip_%=:\n"
      "v_bitop3_b32 v2, v76, v78, s73 bitop3:0x7e\n"
      "v_cmp_ne_u32 vcc, 0, v2\n"
      "s_nop 0\n"
      "s_cbranch_vccz .Lk25_kvskip_%=\n"
      "s_mov_b32 s66, 12288\n"
      "buffer_load_dwordx4 v[76:79], v5, s[36:39], s66 offen sc1\n"
      "s_mov_b32 s46, 1\n"
      ".Lk25_kvskip_%=:\n"
      "v_bitop3_b32 v2, v80, v82, s73 bitop3:0x7e\n"
      "v_cmp_ne_u32 vcc, 0, v2\n"
      "s_nop 0\n"
      "s_cbranch_vccz .Lk26_kvskip_%=\n"
      "s_mov_b32 s66, 16384\n"
      "buffer_load_dwordx4 v[80:83], v5, s[36:39], s66 offen sc1\n"
      "s_mov_b32 s46, 1\n"
      ".Lk26_kvskip_%=:\n"
      "v_bitop3_b32 v2, v84, v86, s73 bitop3:0x7e\n"
      "v_cmp_ne_u32 vcc, 0, v2\n"
      "s_nop 0\n"
      "s_cbranch_vccz .Lk27_kvskip_%=\n"
      "s_mov_b32 s66, 20480\n"
      "buffer_load_dwordx4 v[84:87], v5, s[36:39], s66 offen sc1\n"
      "s_mov_b32 s46, 1\n"
      ".Lk27_kvskip_%=:\n"
      "v_bitop3_b32 v2, v88, v90, s73 bitop3:0x7e\n"
      "v_cmp_ne_u32 vcc, 0, v2\n"
      "s_nop 0\n"
      "s_cbranch_vccz .Lk28_kvskip_%=\n"
      "s_mov_b32 s66, 24576\n"
      "buffer_load_dwordx4 v[88:91], v5, s[36:39], s66 offen sc1\n"
      "s_mov_b32 s46, 1\n"
      ".Lk28_kvskip_%=:\n"
      "v_bitop3_b32 v2, v92, v94, s73 bitop3:0x7e\n"
      "v_cmp_ne_u32 vcc, 0, v2\n"
      "s_nop 0\n"
      "s_cbranch_vccz .Lk29_kvskip_%=\n"
      "s_mov_b32 s66, 28672\n"
      "buffer_load_dwordx4 v[92:95], v5, s[36:39], s66 offen sc1\n"
      "s_mov_b32 s46, 1\n"
      ".Lk29_kvskip_%=:\n"
      "v_bitop3_b32 v2, v96, v98, s73 bitop3:0x7e\n"
      "v_cmp_ne_u32 vcc, 0, v2\n"
      "s_nop 0\n"
      "s_cbranch_vccz .Lk30_kvskip_%=\n"
      "s_mov_b32 s66, 32768\n"
      "buffer_load_dwordx4 v[96:99], v5, s[36:39], s66 offen sc1\n"
      "s_mov_b32 s46, 1\n"
      ".Lk30_kvskip_%=:\n"
      "v_bitop3_b32 v2, v100, v102, s73 bitop3:0x7e\n"
      "v_cmp_ne_u32 vcc, 0, v2\n"
      "s_nop 0\n"
      "s_cbranch_vccz .Lk31_kvskip_%=\n"
      "s_mov_b32 s66, 36864\n"
      "buffer_load_dwordx4 v[100:103], v5, s[36:39], s66 offen sc1\n"
      "s_mov_b32 s46, 1\n"
      ".Lk31_kvskip_%=:\n"
      "v_bitop3_b32 v2, v104, v106, s73 bitop3:0x7e\n"
      "v_cmp_ne_u32 vcc, 0, v2\n"
      "s_nop 0\n"
      "s_cbranch_vccz .Lk32_kvskip_%=\n"
      "s_mov_b32 s66, 40960\n"
      "buffer_load_dwordx4 v[104:107], v5, s[36:39], s66 offen sc1\n"
      "s_mov_b32 s46, 1\n"
      ".Lk32_kvskip_%=:\n"
      "v_bitop3_b32 v2, v108, v110, s73 bitop3:0x7e\n"
      "v_cmp_ne_u32 vcc, 0, v2\n"
      "s_nop 0\n"
      "s_cbranch_vccz .Lk33_kvskip_%=\n"
      "s_mov_b32 s66, 45056\n"
      "buffer_load_dwordx4 v[108:111], v5, s[36:39], s66 offen sc1\n"
      "s_mov_b32 s46, 1\n"
      ".Lk33_kvskip_%=:\n"
      "v_bitop3_b32 v2, v112, v114, s73 bitop3:0x7e\n"
      "v_cmp_ne_u32 vcc, 0, v2\n"
      "s_nop 0\n"
      "s_cbranch_vccz .Lk34_kvskip_%=\n"
      "s_mov_b32 s66, 49152\n"
      "buffer_load_dwordx4 v[112:115], v5, s[36:39], s66 offen sc1\n"
      "s_mov_b32 s46, 1\n"
      ".Lk34_kvskip_%=:\n"
      "v_bitop3_b32 v2, v116, v118, s73 bitop3:0x7e\n"
      "v_cmp_ne_u32 vcc, 0, v2\n"
      "s_nop 0\n"
      "s_cbranch_vccz .Lk35_kvskip_%=\n"
      "s_mov_b32 s66, 53248\n"
      "buffer_load_dwordx4 v[116:119], v5, s[36:39], s66 offen sc1\n"
      "s_mov_b32 s46, 1\n"
      ".Lk35_kvskip_%=:\n"
      "v_bitop3_b32 v2, v120, v122, s73 bitop3:0x7e\n"
      "v_cmp_ne_u32 vcc, 0, v2\n"
      "s_nop 0\n"
      "s_cbranch_vccz .Lk36_kvskip_%=\n"
      "s_mov_b32 s66, 57344\n"
      "buffer_load_dwordx4 v[120:123], v5, s[36:39], s66 offen sc1\n"
      "s_mov_b32 s46, 1\n"
      ".Lk36_kvskip_%=:\n"
      "v_bitop3_b32 v2, v124, v126, s73 bitop3:0x7e\n"
      "v_cmp_ne_u32 vcc, 0, v2\n"
      "s_nop 0\n"
      "s_cbranch_vccz .Lk37_kvskip_%=\n"
      "s_mov_b32 s66, 61440\n"
      "buffer_load_dwordx4 v[124:127], v5, s[36:39], s66 offen sc1\n"
      "s_mov_b32 s46, 1\n"
      ".Lk37_kvskip_%=:\n"
      "s_waitcnt vmcnt(0)\n"
      "s_cmp_eq_u32 s46, 0\n"
      "s_cbranch_scc1 .Lk1_kvchk_%=\n"
      "s_sub_u32 s10, s10, 1\n"
      "s_cbranch_scc0 .Lk3_kvslow_%=\n"
      "s_branch .Lk2_kvok_%=\n"
      ".Lk4_helper_%=:\n"
      "s_waitcnt vmcnt(0)\n"
      "s_barrier\n"
      "s_mov_b64 exec, s[8:9]\n"
      "buffer_atomic_swap v8, v7, s[36:39], 0 offen offset:0\n"
      "s_mov_b64 exec, -1\n"
      "s_sub_u32 s46, s43, 28\n"
      "s_lshl2_add_u32 s46, s46, s33\n"
      "s_lshl_b32 s46, s46, 13\n"
      "v_lshl_add_u32 v2, v1, 7, s46\n"
      "s_mov_b32 s66, 0\n"
      "buffer_load_dword v3, v2, s[24:27], s66 offen\n"
      "s_mov_b32 s66, 131072\n"
      "buffer_load_dword v3, v2, s[24:27], s66 offen\n"
      "s_mov_b32 s66, 262144\n"
      "buffer_load_dword v3, v2, s[24:27], s66 offen\n"
      "s_mov_b32 s66, 393216\n"
      "buffer_load_dword v3, v2, s[24:27], s66 offen\n"
      "s_mov_b32 s66, 524288\n"
      "buffer_load_dword v3, v2, s[24:27], s66 offen\n"
      "s_mov_b32 s66, 655360\n"
      "buffer_load_dword v3, v2, s[24:27], s66 offen\n"
      "s_mov_b32 s66, 786432\n"
      "buffer_load_dword v3, v2, s[24:27], s66 offen\n"
      "s_mov_b32 s66, 917504\n"
      "buffer_load_dword v3, v2, s[24:27], s66 offen\n"
      "s_mov_b32 s66, 1048576\n"
      "buffer_load_dword v3, v2, s[24:27], s66 offen\n"
      "s_mov_b32 s66, 1179648\n"
      "buffer_load_dword v3, v2, s[24:27], s66 offen\n"
      "s_mov_b32 s66, 1310720\n"
      "buffer_load_dword v3, v2, s[24:27], s66 offen\n"
      "s_mov_b32 s66, 1441792\n"
      "buffer_load_dword v3, v2, s[24:27], s66 offen\n"
      "s_mov_b32 s66, 1572864\n"
      "buffer_load_dword v3, v2, s[24:27], s66 offen\n"
      ".Lk5_end_%=:\n"
      ".Lcodeend_%=:\n"
      "s_waitcnt vmcnt(0) lgkmcnt(0)\n"
      :
      : "s"(__builtin_amdgcn_kernarg_segment_ptr()), "s"(blockIdx.x), "v"(threadIdx.x)
      : "memory", "vcc", "scc", "v1", "v2", "v3", "v4", "v5", "v6", "v7", "v8", "v9", "v10", "v11", "v12", "v13", "v14", "v15", "v16",
        "v17", "v18", "v19", "v20", "v21", "v22", "v23", "v24", "v25", "v26", "v27", "v28", "v29", "v30", "v31", "v32",
        "v33", "v34", "v35", "v36", "v37", "v38", "v39", "v40", "v41", "v42", "v43", "v44", "v45", "v46", "v47", "v48",
        "v49", "v50", "v51", "v52", "v53", "v54", "v55", "v56", "v57", "v58", "v59", "v60", "v61", "v62", "v63", "v64",
        "v65", "v66", "v67", "v68", "v69", "v70", "v71", "v72", "v73", "v74", "v75", "v76", "v77", "v78", "v79", "v80",
        "v81", "v82", "v83", "v84", "v85", "v86", "v87", "v88", "v89", "v90", "v91", "v92", "v93", "v94", "v95", "v96",
        "v97", "v98", "v99", "v100", "v101", "v102", "v103", "v104", "v105", "v106", "v107", "v108", "v109", "v110", "v111", "v112",
        "v113", "v114", "v115", "v116", "v117", "v118", "v119", "v120", "v121", "v122", "v123", "v124", "v125", "v126", "v127", "v128",
        "v129", "v130", "v131", "v132", "v133", "v134", "v135", "v136", "v137", "v138", "v139", "v140", "v141", "v142", "v143", "v144",
        "v145", "v146", "v147", "v148", "v149", "v150", "v151", "v152", "v153", "v154", "v155", "v156", "v157", "v158", "v159", "v160",
        "v161", "v162", "v163", "v164", "v165", "v166", "v167", "v168", "v169", "v170", "v171", "v172", "v173", "v174", "v175", "v176",
        "v177", "v178", "v179", "v180", "v181", "v182", "v183", "v184", "v185", "v186", "v187", "v188", "v189", "v190", "v191", "v192",
        "v193", "v194", "v195", "v196", "v197", "v198", "v199", "v200", "v201", "v202", "v203", "v204", "v205", "v206", "v207", "v208",
        "v209", "v210", "v211", "v212", "v213", "v214", "v215", "v216", "v217", "v218", "v219", "v220", "v221", "v222", "v223", "v224",
        "v225", "v226", "v227", "v228", "v229", "v230", "v231", "v232", "v233", "v234", "v235", "v236", "v237", "v238", "v239", "v240",
        "v241", "v242", "v243", "v244", "v245", "v246", "v247", "v248", "v249", "v250", "v251", "v252", "v253", "v254", "v255", "a0",
        "a1", "a2", "a3", "a4", "a5", "a6", "a7", "a8", "a9", "a10", "a11", "a12", "a13", "a14", "a15", "a16",
        "a17", "a18", "a19", "a20", "a21", "a22", "a23", "a24", "a25", "a26", "a27", "a28", "a29", "a30", "a31", "a32",
        "a33", "a34", "a35", "a36", "a37", "a38", "a39", "a40", "a41", "a42", "a43", "a44", "a45", "a46", "a47", "a48",
        "a49", "a50", "a51", "a52", "a53", "a54", "a55", "a56", "a57", "a58", "a59", "a60", "a61", "a62", "a63", "a64",
        "a65", "a66", "a67", "a68", "a69", "a70", "a71", "a72", "a73", "a74", "a75", "a76", "a77", "a78", "a79", "a80",
        "a81", "a82", "a83", "a84", "a85", "a86", "a87", "a88", "a89", "a90", "a91", "a92", "a93", "a94", "a95", "a96",
        "a97", "a98", "a99", "a100", "a101", "a102", "a103", "a104", "a105", "a106", "a107", "a108", "a109", "a110", "a111", "a112",
        "a113", "a114", "a115", "a116", "a117", "a118", "a119", "a120", "a121", "a122", "a123", "a124", "a125", "a126", "a127", "a128",
        "a129", "a130", "a131", "a132", "a133", "a134", "a135", "a136", "a137", "a138", "a139", "a140", "a141", "a142", "a143", "a144",
        "a145", "a146", "a147", "a148", "a149", "a150", "a151", "a152", "a153", "a154", "a155", "a156", "a157", "a158", "a159", "a160",
        "a161", "a162", "a163", "a164", "a165", "a166", "a167", "a168", "a169", "a170", "a171", "a172", "a173", "a174", "a175", "a176",
        "a177", "a178", "a179", "a180", "a181", "a182", "a183", "a184", "a185", "a186", "a187", "a188", "a189", "a190", "a191", "a192",
        "a193", "a194", "a195", "a196", "a197", "a198", "a199", "a200", "a201", "a202", "a203", "a204", "a205", "a206", "a207", "a208",
        "a209", "a210", "a211", "a212", "a213", "a214", "a215", "a216", "a217", "a218", "a219", "a220", "a221", "a222", "a223", "a224",
        "a225", "a226", "a227", "a228", "a229", "a230", "a231", "a232", "a233", "a234", "a235", "a236", "a237", "a238", "a239", "a240",
        "a241", "a242", "a243", "a244", "a245", "a246", "a247", "a248", "a249", "a250", "a251", "a252", "a253", "a254", "a255", "s8",
        "s9", "s10", "s11", "s12", "s13", "s14", "s15", "s16", "s17", "s18", "s19", "s20", "s21", "s22", "s23", "s24",
        "s25", "s26", "s27", "s28", "s29", "s30", "s31", "s33", "s34", "s35", "s36", "s37", "s38", "s39", "s40", "s41",
        "s42", "s43", "s44", "s45", "s46", "s47", "s48", "s49", "s50", "s51", "s52", "s53", "s54", "s55", "s56", "s57",
        "s58", "s59", "s60", "s61", "s62", "s63", "s64", "s65", "s66", "s67", "s68", "s69", "s70", "s71", "s72", "s73",
        "s74", "s75", "s76", "s77", "s78", "s79", "s80", "s81", "s82", "s83", "s84", "s85", "s86", "s87", "s88", "s89",
        "s90", "s91", "s92", "s93", "s94", "s95", "s96", "s97", "s98", "s99");
}

extern "C" void kernel_launch(void* const* d_in, const int* in_sizes, int n_in, void* d_out, int out_size,
                              void* d_ws, size_t ws_size, hipStream_t stream) {
  (void)in_sizes; (void)n_in; (void)out_size; (void)ws_size;
  (void)hipFuncSetAttribute((const void*)fused_block_kernel, hipFuncAttributeMaxDynamicSharedMemorySize, 163840);
  fused_block_kernel<<<256, 256, 163840, stream>>>(d_in[0], d_in[1], d_in[2], d_in[4], d_in[5], d_in[6], d_in[7],
                                                  d_out, d_ws);
}
```
